```python
import torch

def _softmax(x, axis=-1):
    return torch.softmax(x, dim=axis)

def _gelu(x):
    return torch.nn.functional.gelu(x, approximate='tanh')

def _silu(x):
    return torch.nn.functional.silu(x)

def _sigmoid(x):
    return torch.sigmoid(x)

def _layernorm(x, axis=-1, eps=1e-05):
    m = x.mean(dim=axis, keepdim=True)
    v = x.var(dim=axis, keepdim=True, unbiased=False)
    return (x - m) / torch.sqrt(v + eps)

def _scatter_add(tgt, idx, src, axis=0):
    return tgt.clone().index_add_(axis, idx, src)

def _segment_sum(src, seg, n):
    out = torch.zeros((n,) + src.shape[1:], dtype=src.dtype)
    return out.index_add_(0, seg, src)

def _pad(x, pw):
    flat = tuple((v for pair in reversed(pw) for v in pair))
    return torch.nn.functional.pad(x, flat)
kernel_name = 'standalone__memory_aug_recurrent_mem_tr__prompt'

def setup_inputs():

    _w = lambda a: (lambda t: t.to(torch.bfloat16) if t.is_floating_point() else t)(torch.as_tensor(a))
    B, S, Dm = (1, 2048, 640)
    H, Dh = (32, 128)
    M, seg = (8, 32)
    seg = min(seg, S)
    S = S // seg * seg
    return {'x': _w((lambda *s: torch.randn(*s, dtype=torch.bfloat16) * 0.05)(B, S, Dm)), 'mem0': _w((lambda *s: torch.randn(*s, dtype=torch.bfloat16) * 0.4)(B, M, Dm)), 'Wq': _w((lambda *s: torch.randn(*s, dtype=torch.bfloat16) * 6.4)(Dm, H, Dh) * Dm ** (-0.5)), 'Wk': _w((lambda *s: torch.randn(*s, dtype=torch.bfloat16) * 6.4)(Dm, H, Dh) * Dm ** (-0.5)), 'Wv': _w((lambda *s: torch.randn(*s, dtype=torch.bfloat16) * 0.4)(Dm, H, Dh) * Dm ** (-0.5)), 'Wo': _w((lambda *s: torch.randn(*s, dtype=torch.bfloat16) * 0.4)(H, Dh, Dm) * (H * Dh) ** (-0.5)), 'seg_len': seg}

def reference(x, mem0, Wq, Wk, Wv, Wo, seg_len):
    B, S, Dm = x.shape
    H, Dh = (Wq.shape[1], Wq.shape[2])
    M = mem0.shape[1]
    scale = Dh ** (-0.5)
    n_seg = S // seg_len
    L = M + seg_len
    idx = torch.arange(L)
    mask = (idx[:, None] < M) | (idx[:, None] >= idx[None, :])
    mem = mem0
    outs = []
    for s in range(n_seg):
        xs = x[:, s * seg_len:(s + 1) * seg_len, :].clone()
        h = torch.concatenate([mem, xs], axis=1)
        q = torch.einsum('btd,dhe->bhte', h, Wq)
        k = torch.einsum('btd,dhe->bhte', h, Wk)
        v = torch.einsum('btd,dhe->bhte', h, Wv)
        scores = torch.einsum('bhqe,bhke->bhqk', q, k) * scale
        scores = torch.where(mask[None, None], scores, -1000000000.0)
        p = _softmax(scores, axis=-1)
        o = torch.einsum('bhqk,bhke->bhqe', p, v)
        o = torch.einsum('bhte,hed->btd', o, Wo)
        mem = o[:, :M, :].clone()
        outs.append(o[:, M:, :])
    return torch.concatenate(outs, axis=1)
```

```cpp
#include <hip/hip_runtime.h>
#include <hip/hip_bf16.h>
#include <stdint.h>

typedef unsigned short u16;
__device__ __forceinline__ float bf2f(u16 v){ return __uint_as_float(((uint32_t)v)<<16); }
__device__ __forceinline__ u16 f2bf(float f){ uint32_t u=__float_as_uint(f); u += 0x7fff + ((u>>16)&1); return (u16)(u>>16); }
__device__ __forceinline__ float rbf(float f){ return bf2f(f2bf(f)); }

#define DM 640
#define NH 32
#define DH 128
#define HD 4096
#define MM 8
#define SEG 32
#define LL 40

__global__ void nv_gemm(const u16* __restrict__ A, const u16* __restrict__ W, u16* __restrict__ C, int K, int N){
  int r = blockIdx.y; int c = blockIdx.x*blockDim.x+threadIdx.x;
  if(c>=N) return;
  float acc=0.f;
  for(int k=0;k<K;k++) acc += bf2f(A[(size_t)r*K+k])*bf2f(W[(size_t)k*N+c]);
  C[(size_t)r*N+c]=f2bf(acc);
}
__global__ void nv_attn(const u16* __restrict__ Q, const u16* __restrict__ Km, const u16* __restrict__ Vm,
                        const u16* __restrict__ Kx, const u16* __restrict__ Vx, u16* __restrict__ O, int mode, int seg0){
  __shared__ float p[LL];
  int h=blockIdx.x; int r=blockIdx.y; int t=threadIdx.x;
  int s = mode==0 ? seg0 : r/SEG;
  int i = mode==0 ? r : MM + (r%SEG);
  const u16* q = Q + (size_t)r*HD + h*DH;
  const u16* km = Km + (size_t)s*MM*HD + h*DH;
  const u16* vm = Vm + (size_t)s*MM*HD + h*DH;
  const u16* kx = Kx + (size_t)s*SEG*HD + h*DH;
  const u16* vx = Vx + (size_t)s*SEG*HD + h*DH;
  if(t<LL){
    const u16* k = t<MM ? km + (size_t)t*HD : kx + (size_t)(t-MM)*HD;
    float acc=0.f;
    for(int e=0;e<DH;e++) acc += bf2f(q[e])*bf2f(k[e]);
    float sc = rbf(rbf(acc)*0.08838834764831845f);
    if(!(i<MM || i>=t)) sc = rbf(-1e9f);
    p[t]=sc;
  }
  __syncthreads();
  float m=-3e38f; for(int j=0;j<LL;j++) m=fmaxf(m,p[j]);
  float sum=0.f; for(int j=0;j<LL;j++) sum+=__expf(p[j]-m);
  float acc=0.f;
  for(int j=0;j<LL;j++){
    float pj=rbf(__expf(p[j]-m)/sum);
    const u16* v = j<MM ? vm + (size_t)j*HD : vx + (size_t)(j-MM)*HD;
    acc += pj*bf2f(v[t]);
  }
  O[(size_t)r*HD+h*DH+t]=f2bf(acc);
}

static void naive_launch(void* const* d_in, void* d_out, void* d_ws, hipStream_t stream){
  const u16* x=(const u16*)d_in[0]; const u16* mem0=(const u16*)d_in[1];
  const u16* Wq=(const u16*)d_in[2]; const u16* Wk=(const u16*)d_in[3]; const u16* Wv=(const u16*)d_in[4]; const u16* Wo=(const u16*)d_in[5];
  const int S=2048, NS=64;
  u16* ws=(u16*)d_ws;
  u16* Q=ws; u16* K=Q+(size_t)S*HD; u16* V=K+(size_t)S*HD; u16* O=V+(size_t)S*HD;
  u16* mem=O+(size_t)S*HD;
  u16* Qm=mem+65*MM*DM; u16* Km=Qm+(size_t)MM*HD; u16* Vm=Km+(size_t)NS*MM*HD; u16* Om=Vm+(size_t)NS*MM*HD;
  nv_gemm<<<dim3(HD/256,S),256,0,stream>>>(x,Wq,Q,DM,HD);
  nv_gemm<<<dim3(HD/256,S),256,0,stream>>>(x,Wk,K,DM,HD);
  nv_gemm<<<dim3(HD/256,S),256,0,stream>>>(x,Wv,V,DM,HD);
  for(int s=0;s<NS;s++){
    const u16* m = s==0? mem0 : mem+(size_t)s*MM*DM;
    nv_gemm<<<dim3(HD/256,MM),256,0,stream>>>(m,Wq,Qm,DM,HD);
    nv_gemm<<<dim3(HD/256,MM),256,0,stream>>>(m,Wk,Km+(size_t)s*MM*HD,DM,HD);
    nv_gemm<<<dim3(HD/256,MM),256,0,stream>>>(m,Wv,Vm+(size_t)s*MM*HD,DM,HD);
    nv_attn<<<dim3(NH,MM),128,0,stream>>>(Qm,Km,Vm,K,V,Om,0,s);
    nv_gemm<<<dim3(DM/64,MM),64,0,stream>>>(Om,Wo,mem+(size_t)(s+1)*MM*DM,HD,DM);
  }
  nv_attn<<<dim3(NH,S),128,0,stream>>>(Q,Km,Vm,K,V,O,1,0);
  nv_gemm<<<dim3(DM/64,S),64,0,stream>>>(O,Wo,(u16*)d_out,HD,DM);
}
extern "C" void kernel_launch(void* const* d_in, const int* in_sizes, int n_in,
                              void* d_out, int out_size, void* d_ws, size_t ws_size,
                              hipStream_t stream) {
  naive_launch(d_in,d_out,d_ws,stream);
}
```

```cpp
#include <hip/hip_runtime.h>
#include <stdint.h>

typedef unsigned short u16;
typedef unsigned u32;
typedef int v4i __attribute__((ext_vector_type(4)));
typedef short v4s __attribute__((ext_vector_type(4)));
typedef u32 v2u __attribute__((ext_vector_type(2)));
typedef u32 v4u __attribute__((ext_vector_type(4)));
typedef u32 v8u __attribute__((ext_vector_type(8)));
typedef u32 v16u __attribute__((ext_vector_type(16)));
typedef u32 v32u __attribute__((ext_vector_type(32)));
typedef __bf16 bf8 __attribute__((ext_vector_type(8)));
typedef __bf16 bf16x16 __attribute__((ext_vector_type(16)));
typedef __bf16 bf2 __attribute__((ext_vector_type(2)));
typedef float f4 __attribute__((ext_vector_type(4)));
#define LAS __attribute__((address_space(3)))
typedef LAS unsigned char* lptr;
typedef __amdgpu_buffer_rsrc_t rsrc_t;
#define DEV __device__ __forceinline__
#define SC1 16

#define MEM_SLOT 32768
#define P_SLOT 49152
#define OFF_P 131072
#define OFF_CT 327680
#define OFF_CTRL 393216
constexpr int OFF_FLG = OFF_CTRL;
static_assert(OFF_P == 4 * MEM_SLOT && OFF_CT == OFF_P + 4 * P_SLOT && OFF_CTRL == OFF_CT + 65536, "ws");
constexpr int OFF_POL = OFF_FLG + 512;
constexpr int OFF_FQ = OFF_FLG + 1024;
constexpr int OFF_FZ = OFF_FQ + 8192;
constexpr int OFF_FP = OFF_FZ + 8192;
constexpr int OFF_FY = OFF_FP + 32768;
constexpr int OFF_ZM = OFF_FLG + 65536;
constexpr int OFF_RDY = OFF_FLG + 64512, OFF_E = OFF_FLG + 64000;
constexpr int CLR_A = OFF_FLG / 16, CLR_N = CLR_A + (51200 - 1024) / 16;
constexpr int OFF_END = OFF_ZM + 119 * 2097152;
constexpr int OFF_DBG = 244 << 20;
static_assert(OFF_END < OFF_DBG, "ws");

constexpr int LDS_STAGE = 65536, STAGE_BUF = 40960;
#define NQ 3
constexpr int LDS_BYTES = NQ == 3 ? 163840 : LDS_STAGE + 2 * STAGE_BUF;
constexpr int LDS_MEM = 0;
constexpr int LDS_TM = 10240;
constexpr int LDS_TX = 16384;
constexpr int LDS_VM = 10240;
constexpr int LDS_X0 = 0, LDS_X1 = 65536;
constexpr int LDS_V = 40960;

struct Args { const u16* x; const u16* mem0; const u16* wq; const u16* wk; const u16* wv; const u16* wo; u16* out; unsigned char* ws; };
#define TA 59
#define TB 85
#define TD 8
#define TG 70
#define DV 14
#define OT 8900
#define OD 108
#define OS 6
#define OX 0
#define OAUX 16
#define OAUX2 0
#define O1 2000
#define OP 24
#define OQ 50
#define OJ 16
#define OW 20
#define OL 600
#define TH 42
#define T2 16
#define TQ 36
#define TZ 36
#define TP 16
#define D0 20
#define DQ 8
#define TL 31
#define XX 12
#define ZS 800
#define G1 900

DEV u32 pkbf(float lo, float hi) { bf2 r = {(__bf16)lo, (__bf16)hi}; return __builtin_bit_cast(u32, r); }
DEV u32 rt32() { return (u32)__builtin_amdgcn_s_memrealtime(); }
DEV u32 ck32() { return (u32)__builtin_amdgcn_s_memtime(); }
DEV float swapadd32(float a, float b) { auto r = __builtin_amdgcn_permlane32_swap(__float_as_uint(a), __float_as_uint(b), false, false); return __uint_as_float(r[0]) + __uint_as_float(r[1]); }
DEV float swapadd16(float a, float b) { auto r = __builtin_amdgcn_permlane16_swap(__float_as_uint(a), __float_as_uint(b), false, false); return __uint_as_float(r[0]) + __uint_as_float(r[1]); }
DEV float allmax(float m) {
  m = fmaxf(m, __uint_as_float(__builtin_amdgcn_update_dpp(0, __float_as_uint(m), 0x128, 0xf, 0xf, false)));
  auto r = __builtin_amdgcn_permlane16_swap(__float_as_uint(m), __float_as_uint(m), false, false);
  m = fmaxf(__uint_as_float(r[0]), __uint_as_float(r[1]));
  auto q = __builtin_amdgcn_permlane32_swap(__float_as_uint(m), __float_as_uint(m), false, false);
  return fmaxf(__uint_as_float(q[0]), __uint_as_float(q[1]));
}
DEV float allsum(float m) {
  m += __uint_as_float(__builtin_amdgcn_update_dpp(0, __float_as_uint(m), 0x128, 0xf, 0xf, false));
  m = swapadd16(m, m);
  return swapadd32(m, m);
}
#define SMF(a, b, c, idx) __builtin_amdgcn_smfmac_f32_16x16x64_bf16(__builtin_bit_cast(bf8, a), __builtin_bit_cast(bf16x16, b), c, idx, 0, 0)
#define MF16(a, b, c) __builtin_amdgcn_mfma_f32_16x16x32_bf16(__builtin_bit_cast(bf8, a), __builtin_bit_cast(bf8, b), c, 0, 0, 0)
#define FENCE() asm volatile("" ::: "memory")
#define PINS(x) asm volatile("" : "+s"(x))
#define UNI(x) __builtin_amdgcn_readfirstlane(x)
#define PINV(x) asm volatile("" : "+v"(x))
#define BAR() asm volatile("s_waitcnt lgkmcnt(0)\n\ts_barrier" ::: "memory")
#define VMW(n) asm volatile("s_waitcnt vmcnt(" #n ")" ::: "memory")
#define LD128(voff, soff) __builtin_amdgcn_raw_buffer_load_b128(rb, voff, soff, SC1)
#define LD64(voff, soff) __builtin_amdgcn_raw_buffer_load_b64(rb, voff, soff, SC1)
#define LD32(voff, soff) __builtin_amdgcn_raw_buffer_load_b32(rb, voff, soff, SC1)
#define ST128(v, voff, soff) __builtin_amdgcn_raw_buffer_store_b128(v, rb, voff, soff, SC1)
#define ST64(v, voff, soff) __builtin_amdgcn_raw_buffer_store_b64(v, rb, voff, soff, SC1)
#define ST32(v, voff, soff) __builtin_amdgcn_raw_buffer_store_b32(v, rb, voff, soff, SC1)
#define ST16(v, voff, soff) __builtin_amdgcn_raw_buffer_store_b16(v, rb, voff, soff, SC1)
DEV v4u lo4(const v8u& v) { return __builtin_shufflevector(v, v, 0, 1, 2, 3); }
DEV v4u hi4(const v8u& v) { return __builtin_shufflevector(v, v, 4, 5, 6, 7); }
DEV v8u cat8(v4u a, v4u b) { return __builtin_shufflevector(a, b, 0, 1, 2, 3, 4, 5, 6, 7); }
DEV v4u cat4(v2u a, v2u b) { return __builtin_shufflevector(a, b, 0, 1, 2, 3); }
DEV v4u lds128(int off) { return *(const LAS v4u*)(size_t)off; }
DEV v2u ldstr(int off) { return __builtin_bit_cast(v2u, __builtin_amdgcn_ds_read_tr16_b64_v4i16((LAS v4s*)(size_t)off)); }
DEV v4i mkrs(const void* p, u32 bytes) { const uint64_t a = (uint64_t)p; return (v4i){(int)a, (int)(a >> 32) & 0xffff, (int)bytes, 0x00020000}; }
DEV void dma(v4i rs, int ldsoff, int voff, int soff) { asm volatile("s_mov_b32 m0, %0\n\ts_nop 0\n\tbuffer_load_dwordx4 %1, %2, %3 offen lds" ::"s"(ldsoff), "v"(voff), "s"(rs), "s"(soff) : "memory"); }
DEV void dmac(v4i rs, int ldsoff, int voff, int soff) { asm volatile("s_mov_b32 m0, %0\n\ts_nop 0\n\tbuffer_load_dwordx4 %1, %2, %3 offen sc1 lds" ::"s"(ldsoff), "v"(voff), "s"(rs), "s"(soff) : "memory"); }
DEV void wait_until(u32 t) { for (;;) { const u32 d = t - rt32(); if ((int)d <= 0 || d > 20000u) break; __builtin_amdgcn_s_sleep(8); } }
DEV u32 umax3(u32 a, u32 b, u32 c) { return max(max(a, b), c); }
DEV bool any(bool p) { return __builtin_amdgcn_ballot_w64(p) != 0; }
constexpr int BUDGET = 1 << 15;
template <int SL = 2> DEV u32 poll32(rsrc_t rb, int voff, int soff, int& budget) {
  u32 v;
  for (;;) {
    FENCE();
    v = LD32(voff, soff);
    if (!any(v == 0xFFFFFFFFu)) break;
    if (--budget < 0) break;
    __builtin_amdgcn_s_sleep(SL);
  }
  return v;
}
#define MARK(i) do {} while (0)
#define TS(t)
#define STAT(i, v) do {} while (0)

struct StA { int v0, v1, ld; };
DEV StA stA_init(int w, int lane, int h) {
  const int q = lane >> 4, base = (16 * (w & 3) + q) * 8192 + h * 256;
  return StA{base + (((lane & 15) ^ (2 * q)) << 4), base + (((lane & 15) ^ (2 * q + 8)) << 4), LDS_STAGE + (w >> 2) * 16384 + (w & 3) * 4096};
}
DEV void stA_issue(const StA& st, v4i rs, int row0, int off) {
#pragma unroll
  for (int i = 0; i < 4; i++) dma(rs, st.ld + off + i * 1024, i < 2 ? st.v0 : st.v1, (row0 + 4 * i) * 8192);
}
DEV int stA_base(int lane, int c0) {
  const int gb = lane >> 4, q = (lane >> 2) & 3, p = lane & 3;
  return LDS_STAGE + (8 * gb + q) * 256 + (((c0 + (p >> 1)) ^ (2 * (q | ((gb & 1) << 2)))) << 4) + (p & 1) * 8;
}
DEV v8u stA_read(int base, int off) {
  return cat8(cat4(ldstr(base + off), ldstr(base + off + 1024)), cat4(ldstr(base + off + 8192), ldstr(base + off + 9216)));
}
DEV int fB(int row) { return 2 * ((row & 1) | (((row >> 2) & 1) << 1) | ((((row >> 3) ^ (row >> 4)) & 1) << 2)); }
struct StB { int v[5]; };
DEV StB stB_init(int w, int lane, int h) {
  StB st;
#pragma unroll
  for (int i = 0; i < 5; i++) { const int L = (5 * w + i) * 64 + lane, row = L / 80, c = L - row * 80; st.v[i] = (h * 128 + row) * 1280 + ((c ^ fB(row)) << 4); }
  return st;
}
DEV void stB_issue(const StB& st, v4i rs, int w, int r, int buf) {
#pragma unroll
  for (int i = 0; i < 5; i++) dma(rs, LDS_STAGE + buf * STAGE_BUF + (5 * w + i) * 1024, st.v[i], r * 40960);
}
DEV int stB_addr(int row, int c0, int lane) { const int p = lane & 3; return LDS_STAGE + row * 1280 + (((c0 + (p >> 1)) ^ fB(row)) << 4) + (p & 1) * 8; }
#define STAGE_WAIT(n) asm volatile("s_waitcnt vmcnt(%0)\n\ts_barrier" ::"n"(n) : "memory")
DEV void x_issue(v4i rx, int w, int xv, int seg, int ldsbase) {
#pragma unroll
  for (int i = 0; i < 5; i++) dma(rx, ldsbase + (5 * w + i) * 1024, xv, seg * 40960 + i * 64);
}
DEV int x_lane(int w, int lane) { return ((w >> 2) * 16 + (lane & 15)) * 1280 + (lane >> 4) * 16 + (w & 3) * 320; }
struct Acc { f4 c[4]; };
DEV void first_step(Acc& t, const v8u& wa, const v8u& wb, int kk, int lane) {
  const int xb = LDS_X0 + lane * 16 + kk * 2048;
  const v4u x0 = lds128(xb), x1 = lds128(xb + 20480), x2 = lds128(xb + 1024), x3 = lds128(xb + 21504);
  t.c[0] = MF16(lo4(wa), x0, t.c[0]); t.c[1] = MF16(lo4(wb), x0, t.c[1]); t.c[2] = MF16(lo4(wa), x1, t.c[2]); t.c[3] = MF16(lo4(wb), x1, t.c[3]);
  t.c[0] = MF16(hi4(wa), x2, t.c[0]); t.c[1] = MF16(hi4(wb), x2, t.c[1]); t.c[2] = MF16(hi4(wa), x3, t.c[2]); t.c[3] = MF16(hi4(wb), x3, t.c[3]);
}
DEV v4u first_out(const f4& p, const f4& q) { return (v4u){pkbf(p[0], q[0]), pkbf(p[1], q[1]), pkbf(p[2], q[2]), pkbf(p[3], q[3])}; }

DEV void load_qk(const Args& a, int h, int w, int lane, v8u (&W)[2][10], v4u& o0, v4u& o1) {
  const v4i rs = mkrs(w < 4 ? a.wq : a.wk, 640 * 8192);
  const StA st = stA_init(w, lane, h);
  const int b0 = stA_base(lane, 4 * (w & 3)) + (w >> 2) * 16384, b1 = stA_base(lane, 4 * (w & 3) + 2) + (w >> 2) * 16384;
  Acc t = {};
#pragma unroll
  for (int r = 0; r < NQ; r++) stA_issue(st, rs, 64 * r, r * 32768);
#pragma unroll
  for (int r = 0; r < 10; r++) {
    STAGE_WAIT(4 * (9 - r < NQ - 1 ? 9 - r : NQ - 1));
    W[0][r] = stA_read(b0, (r % NQ) * 32768);
    W[1][r] = stA_read(b1, (r % NQ) * 32768);
    BAR();
    if (r + NQ < 10) stA_issue(st, rs, 64 * (r + NQ), (r % NQ) * 32768);
    first_step(t, W[0][r], W[1][r], r, lane);
  }
  o0 = first_out(t.c[0], t.c[1]); o1 = first_out(t.c[2], t.c[3]);
}

DEV v4u mem0_load(const Args& a, int L) {
  const u32* p = (const u32*)(a.mem0 + (L & 7) * 640 + (L >> 4) * 16 + ((L >> 3) & 1) * 2);
  return (v4u){p[0], p[2], p[4], p[6]};
}
DEV void mem0_put(v4u v, int L) {
  PINV(v);
  *(LAS v4u*)(size_t)(LDS_MEM + L * 16) = (v4u){min(v.x, 0xFFFEFFFFu), min(v.y, 0xFFFEFFFFu), min(v.z, 0xFFFEFFFFu), min(v.w, 0xFFFEFFFFu)};
}

DEV v2u marked(rsrc_t rb, v2u v, v2u mk, int voff, int soff) {
  for (int budget = BUDGET; any((v.x & ~7u) != mk.x || v.y != mk.y) && --budget >= 0;) { __builtin_amdgcn_s_sleep(2); FENCE(); v = LD64(voff, soff); }
  return v;
}
DEV v2u mark(v2u e) { const uint64_t t = ((((uint64_t)e.y << 32) | e.x) + 8) & ~7ull; return (v2u){(u32)t, (u32)(t >> 32)}; }
DEV v4u pol_load(const Args& a) {
  const rsrc_t rb = __builtin_amdgcn_make_buffer_rsrc(a.ws, 0, 0x0fc00000, 0x00020000);
  FENCE();
  const v2u e = LD64(0, OFF_E);
  return cat4(LD64(min((int)threadIdx.x & 63, 39) * 8, OFF_POL), e);
}
typedef uint64_t PM;
DEV PM pol_get(const Args& a, v4u p) {
  const rsrc_t rb = __builtin_amdgcn_make_buffer_rsrc(a.ws, 0, 0x0fc00000, 0x00020000);
  const v2u v = marked(rb, (v2u){p.x, p.y}, mark((v2u){p.z, p.w}), min((int)threadIdx.x & 63, 39) * 8, OFF_POL);
  const uint64_t m = __builtin_amdgcn_ballot_w64(v.x & 1u);
  return (m & 0x3FFFFFFFEull) | (uint64_t)((u32)(__builtin_popcount((u32)(m >> 32) & 0xF0u) + ((u32)m & 1u) + 5) >> 3) | __builtin_amdgcn_ballot_w64(v.x & 2u) << 40;
}
DEV PM get_pol(const Args& a) { return pol_get(a, pol_load(a)); }
DEV int near13(PM pm, int p) { return (int)((pm >> p) & 1) << 13; }
DEV void clear_words(const Args& a, int k) {
  const rsrc_t rb = __builtin_amdgcn_make_buffer_rsrc(a.ws, 0, 0x0fc00000, 0x00020000);
  const v4u on = {~0u, ~0u, ~0u, ~0u};
  const v2u e = LD64(0, OFF_E);
  for (int i = k * 512 + threadIdx.x; i < CLR_N; i += 24 * 512) ST128(on, i * 16 + (i >= CLR_A ? 1024 : 0), 0);
  asm volatile("s_waitcnt vmcnt(0)\n\ts_barrier" ::: "memory");
  if (threadIdx.x == 0) ST64(mark(e), 0, OFF_RDY + k * 8);
}
#define GSL 4
DEV void gate(const Args& a, u32 from, int after) {
  const rsrc_t rb = __builtin_amdgcn_make_buffer_rsrc(a.ws, 0, 0x0fc00000, 0x00020000);
  const int lane = threadIdx.x & 63;
  int budget = BUDGET;
  wait_until(from);
  poll32<GSL>(rb, (lane & 31) * 4 + (lane >> 5) * (OFF_FY - OFF_FZ), OFF_FZ, budget);
  const u32 t = rt32() + after;
  while ((int)(t - rt32()) > 0) __builtin_amdgcn_s_sleep(1);
}
#define CSN 10
DEV int zx_off(int s, int t, int h) {
  const int v = (t * 26) >> 8, b = s * 4 + v, p = (b * 171) >> 9;
  return OFF_ZM + p * 2097152 + ((b - 3 * p) * 40 + (t - 10 * v) * 4 + (h >> 3)) * 16384 + (h & 7) * 1024 + (h & 8) * 1024;
}
DEV int qkx_off(int s, int h, PM pm) { return OFF_ZM + (86 + (s >> 1)) * 2097152 + ((s & 1) * 64 + h * 2) * 16384 + near13(pm, 1 + (s >> 1)); }
DEV int pt_off(int s, PM pm) { const int p = (s * 43) >> 8, n = near13(pm, p + 40); return OFF_ZM + p * 2097152 + (s - 6 * p) * 327680 + n * 2 + (n ^ 8192); }
DEV int zm_off(int s, PM pm) { return s < 3 ? OFF_ZM + 118 * 2097152 + s * 655360 + near13(pm, 33) : qkx_off(s - 3, 0, pm); }
DEV void calibrate(const Args& a, int z) {
  const rsrc_t rb = __builtin_amdgcn_make_buffer_rsrc(a.ws, 0, 0x0fc00000, 0x00020000);
  const v4i rs = mkrs(a.ws, 0x0fc00000);
  const int tid = threadIdx.x, lane = tid & 63, w = __builtin_amdgcn_readfirstlane(tid >> 6);
  const int p = w >= 6 && z < 11 ? 1 + z : z && z < 34 ? 86 + z : z == 34 || z == 35 ? z - 22 : 0;
  const int base = __builtin_amdgcn_readfirstlane((p ? OFF_ZM + (p - 1) * 2097152 + 1966080 : z ? (z - 36) * MEM_SLOT + 16384 : OFF_ZM - 16384) + 2048 + w * 768);
  if (lane < CSN) ST32(0u, (lane & 1) * 8192 + (lane >> 1) * 128, base);
  VMW(0);
  u32 lat[2] = {~0u, ~0u};
#pragma unroll
  for (int i = 0; i < CSN; i++) {
    uint64_t t0, t1; u32 v;
    asm volatile("s_memtime %0\n\ts_waitcnt lgkmcnt(0)\n\tbuffer_load_dword %2, off, %3, %4 sc1\n\ts_waitcnt vmcnt(0)\n\ts_memtime %1\n\ts_waitcnt lgkmcnt(0)"
                 : "=&s"(t0), "=&s"(t1), "=&v"(v) : "s"(rs), "s"(base + (i & 1) * 8192 + (i >> 1) * 128) : "memory");
    if ((u32)(t1 - t0) > 320u) lat[i & 1] = min(lat[i & 1], (u32)(t1 - t0));
  }
  if (lane == 0) *(LAS v2u*)(size_t)(131072 + w * 8) = (v2u){lat[0], lat[1]};
  BAR();
  if (w == 0) {
    u32 l0[2] = {~0u, ~0u}, l1[2] = {~0u, ~0u};
#pragma unroll
    for (int i = 0; i < 8; i++) { const v2u r = *(const LAS v2u*)(size_t)(131072 + i * 8); l0[i / 6] = min(l0[i / 6], r.x); l1[i / 6] = min(l1[i / 6], r.y); }
    if (z >= 11) { l0[0] = min(l0[0], l0[1]); l1[0] = min(l1[0], l1[1]); }
    const v2u mk = mark(LD64(0, OFF_E));
    marked(rb, LD64(min(lane, 23) * 8, OFF_RDY), mk, min(lane, 23) * 8, OFF_RDY);
    if (lane == 0) ST64(((v2u){mk.x | (l1[0] < l0[0] ? 1u : 0u) | (l1[1] < l0[1] ? 2u : 0u), mk.y}), 0, OFF_POL + z * 8);
  }
}
DEV v32u cat32(const v8u& a, const v8u& b, const v8u& c, const v8u& d) {
  const v16u x = __builtin_shufflevector(a, b, 0, 1, 2, 3, 4, 5, 6, 7, 8, 9, 10, 11, 12, 13, 14, 15);
  const v16u y = __builtin_shufflevector(c, d, 0, 1, 2, 3, 4, 5, 6, 7, 8, 9, 10, 11, 12, 13, 14, 15);
  return __builtin_shufflevector(x, y, 0, 1, 2, 3, 4, 5, 6, 7, 8, 9, 10, 11, 12, 13, 14, 15, 16, 17, 18, 19, 20, 21, 22, 23, 24, 25, 26, 27, 28, 29, 30, 31);
}
#define STR_(x) #x
#define STR(x) STR_(x)
#define N "\n\t"
#define V10(p) "v" #p "0", "v" #p "1", "v" #p "2", "v" #p "3", "v" #p "4", "v" #p "5", "v" #p "6", "v" #p "7", "v" #p "8", "v" #p "9"
#define S10(p) "s" #p "0", "s" #p "1", "s" #p "2", "s" #p "3", "s" #p "4", "s" #p "5", "s" #p "6", "s" #p "7", "s" #p "8", "s" #p "9"
#define CLOB_S S10(4), S10(5), S10(6), S10(7), S10(8), "vcc", "scc", "memory"
#define CLOB_V16_95 "v14", "v15", "v16", "v17", "v18", "v19", V10(2), V10(3), V10(4), V10(5), V10(6), V10(7), V10(8), "v90", "v91", "v92", "v93", "v94", "v95"
#define A_TS(r)
#define A_STAT(t)
#define A_FAIL(r)
#define A_LATE
#define A_GAP
#define A_ARR "s_waitcnt vmcnt(0)" N
#define A_TSA
#define A_TSB A_TS(80)
#define A_WAIT(L, E) #L ":" N "s_memrealtime s[58:59]" N "s_waitcnt lgkmcnt(0)" N A_GAP "s_sub_u32 s47, s60, s58" N "s_sub_u32 s59, s47, 4" N "s_cmp_lt_u32 s59, 20000" N "s_cbranch_scc1 " #L "b" N \
  "s_sub_u32 s47, s47, 1" N "s_cmp_gt_u32 s47, 2" N "s_cbranch_scc1 " #E "f" N "s_nop 3" N "s_cmp_eq_u32 s47, 0" N "s_cbranch_scc1 " #E "f" N "s_nop 3" N \
  "s_cmp_eq_u32 s47, 1" N "s_cbranch_scc1 " #E "f" N "s_nop 5" N #E ":" N
#define A_POLL(L1, L2, V, VO, SO) #L1 ":" N "v_cmp_eq_u32 vcc, -1, " V N "s_cbranch_vccz " #L2 "f" N "s_sub_u32 s45, s45, 1" N "s_cmp_lt_i32 s45, 0" N "s_cbranch_scc1 " #L2 "f" N \
  "s_sleep 1" N "buffer_load_dword " V ", " VO ", %[rs], " SO " sc1" N "s_waitcnt vmcnt(0)" N "s_branch " #L1 "b" N #L2 ":" N
#define A_INIT "s_mov_b32 s45, 0x8000" N "s_mov_b32 s46, 0x8000" N "s_mov_b32 s70, 0" N "s_mov_b32 s71, 0" N
#define A_CT(d, c) "s_and_b32 " #d ", s40, 3" N "s_lshl_b32 " #d ", " #d ", 14" N "s_add_u32 " #d ", " #d ", " c N
#define A_LATEST(V, D, T) "s_memrealtime s[58:59]" N "s_waitcnt lgkmcnt(0)" N "v_subrev_u32 " V ", s58, " V N \
  "s_nop 1" N "v_max_i32_dpp " V ", " V ", " V " row_ror:1 row_mask:0xf bank_mask:0xf" N "s_nop 1" N "v_max_i32_dpp " V ", " V ", " V " row_ror:2 row_mask:0xf bank_mask:0xf" N \
  "s_nop 1" N "v_max_i32_dpp " V ", " V ", " V " row_ror:4 row_mask:0xf bank_mask:0xf" N "s_nop 1" N "v_max_i32_dpp " V ", " V ", " V " row_ror:8 row_mask:0xf bank_mask:0xf" N "s_nop 0" N \
  "v_readlane_b32 s47, " V ", 0" N "v_readlane_b32 s59, " V ", 16" N "v_readlane_b32 s48, " V ", 32" N "v_readlane_b32 s49, " V ", 48" N \
  "s_max_i32 s47, s47, s59" N "s_max_i32 s48, s48, s49" N "s_max_i32 s47, s47, s48" N "s_add_u32 " D ", s58, s47" N "s_add_u32 " D ", " D ", " T N
#define A_TSET "s_mov_b32 s67, s61" N A_CT(s61, "%[wk]") "s_add_u32 s61, s61, " STR(OFF_CT) N
#define A_ASET A_CT(s66, "%[wk]") "s_xor_b32 s68, s66, 32768" N "s_add_u32 s66, s66, %[own]" N "s_add_u32 s68, s68, %[own]" N
#define A_PEN(D, T, P) "s_sub_u32 s47, " D ", " T N "s_sub_u32 s47, s47, " P N "s_cmp_gt_i32 s47, %[th]" N "s_cselect_b32 s47, %[tp], 0" N
#define A_TIMER A_ARR "v_lshlrev_b32 v14, 3, %[la]" N "s_mov_b32 s63, s60" N "s_memrealtime s[58:59]" N "s_waitcnt lgkmcnt(0)" N "s_add_u32 s60, s58, %[t2]" N \
  "31:" N A_WAIT(33, 34) "buffer_load_dword v15, v14, %[rs], s67 offen sc1" N "s_add_u32 s60, s60, %[tq]" N "s_waitcnt vmcnt(0)" N "v_cmp_eq_u32 vcc, -1, v15" N "s_cbranch_vccz 32f" N \
  "s_sub_u32 s45, s45, 1" N "s_cmp_lt_i32 s45, 0" N "s_cbranch_scc0 31b" N "32:" N A_LATEST("v15", "s60", "%[tt]") A_PEN("s60", "%[tt]", "s63") "s_add_u32 s60, s60, s47" N A_WAIT(37, 38) A_LATE
#define A_ANN0 "s_memrealtime s[58:59]" N
#define A_ANN1 "s_sub_u32 s47, s58, %[dv]" N "s_min_u32 s47, s47, -3" N "v_mov_b32 v14, s47" N "s_mov_b32 exec_hi, 0" N "buffer_store_dword v14, %[g2], %[rs], s66 offen sc1" N "s_mov_b32 exec_hi, -1" N
#define A_ANN2 "v_mov_b32 v14, -1" N "s_mov_b32 exec_hi, 0" N "buffer_store_dword v14, %[g2], %[rs], s68 offen sc1" N "s_mov_b32 exec_hi, -1" N
#define A_RETR "v_mov_b32 v14, -1" N "s_mov_b32 exec_hi, 0" N "buffer_store_dword v14, %[g2], %[rs], s66 offen sc1" N "s_mov_b32 exec_hi, -1" N
#define A_SLOT "s_and_b32 s47, s40, 3" N "s_lshl_b32 s41, s47, 15" N "s_add_u32 s41, s41, %[wk]" N
#define A_SLOT2 "s_add_u32 s42, s41, 16384-6144" N "s_add_u32 s62, %[mw], 2048" N
#define A_W0 "s_waitcnt vmcnt(0) lgkmcnt(0)" N
#define A_W1 A_ANN0 "s_waitcnt lgkmcnt(0)" N "s_add_u32 s58, s58, 38" N "s_waitcnt vmcnt(0)" N
#define A_WL "s_waitcnt lgkmcnt(0)" N
#define A_WV "s_waitcnt vmcnt(0)" N
#define A_DMA2 "s_mov_b32 m0, s62" N "s_nop 0" N "buffer_load_dwordx4 %[gv], %[rs], s42 offen sc1 lds" N
#define A_GATHER(DMA2, MID) "s_barrier" N A_TS(72) "buffer_load_dwordx4 %[gv], %[rs], s41 offen sc1 lds" N DMA2 \
  "12:" N MID "s_barrier" N A_TS(74) \
  ".set ii,0" N ".rept 4" N "ds_read_b128 v[56+4*ii:59+4*ii], %[la] offset:1024*ii" N ".set ii,ii+1" N ".endr" N
#define A_GATHER2(MIDB, ANN0) MIDB "s_barrier" N A_TSA ANN0 \
  ".rept 6" N "ds_read_b128 v[56+4*ii:59+4*ii], %[la] offset:1024*ii" N ".set ii,ii+1" N ".endr" N
#define A_S0(k) "v_smfmac_f32_16x16x64_bf16 v[16:19], v[56+4*" #k ":59+4*" #k "], v[96+8*" #k ":103+8*" #k "], %[idx]" N
#define A_S1(k) "v_smfmac_f32_16x16x64_bf16 v[20:23], v[56+4*" #k ":59+4*" #k "], v[176+8*" #k ":183+8*" #k "], %[idx]" N
#define A_LW(n) "s_waitcnt lgkmcnt(" #n ")" N
#define A_ZERO "s_mov_b32 m0, %[mw]" N "v_mov_b64 v[16:17], 0" N "v_mov_b64 v[18:19], 0" N "v_mov_b64 v[20:21], 0" N "v_mov_b64 v[22:23], 0" N "s_waitcnt lgkmcnt(0)" N
#define A_RETRY(RETR) "19:" N "v_mov_b32 v18, 0" N ".set ii,0" N ".rept 20" N "v_max3_u32 v18, v18, v[56+2*ii], v[57+2*ii]" N ".set ii,ii+1" N ".endr" N \
  "v_cmp_eq_u32 vcc, -1, v18" N "s_cbranch_vccz 13b" N A_FAIL(s70) "s_sub_u32 s46, s46, 1" N "s_cmp_lt_i32 s46, 0" N "s_cbranch_scc1 13b" N RETR A_ZERO "s_branch 11b" N \
  "20:" N "s_barrier" N "s_branch 12b" N
#define A_KXREAD ".set ii,0" N ".rept 4" N "ds_read_b128 v[24+8*ii:27+8*ii], %[kxa] offset:2048*ii" N "ds_read_b128 v[28+8*ii:31+8*ii], %[kxa] offset:2048*ii+256" N ".set ii,ii+1" N ".endr" N
#define QK_CORE(SETUP, TIMER, DMA2, MID, MIDB, ANN0, ANN1, TX) \
  "10:" N A_SLOT SETUP \
  A_ZERO "s_cmp_eq_u32 s40, 0" N "s_cbranch_scc1 20f" N TIMER "11:" N A_GATHER(DMA2, MID) \
  A_LW(3) A_S0(0) A_S1(0) A_LW(2) A_S0(1) A_S1(1) A_GATHER2(MIDB, ANN0) TX A_LW(7) A_S0(2) A_S1(2) A_LW(6) A_S0(3) A_S1(3) A_LW(5) ANN1 A_S0(4) A_S1(4) \
  A_LW(4) A_S0(5) A_S1(5) A_LW(3) A_S0(6) A_S1(6) A_LW(2) A_S0(7) A_S1(7) A_LW(1) A_S0(8) A_S1(8) A_LW(0) A_S0(9) A_S1(9) "s_nop 6" N \
  "v_permlane32_swap_b32 v16, v18" N "v_permlane32_swap_b32 v17, v19" N "v_permlane32_swap_b32 v20, v22" N "v_permlane32_swap_b32 v21, v23" N \
  "v_pk_add_f32 v[16:17], v[16:17], v[18:19]" N "v_pk_add_f32 v[20:21], v[20:21], v[22:23]" N "v_cmp_u_f32 vcc, v16, v17" N \
  "v_cvt_pk_bf16_f32 v16, v16, v20" N "v_cvt_pk_bf16_f32 v17, v17, v21" N \
  "ds_write2_b32 %[wr], v16, v17 offset1:4" N "s_cbranch_vccnz 19f" N "13:" N A_TSB "s_cmp_eq_u32 s40, 63" N "s_waitcnt lgkmcnt(0)" N "s_barrier" N A_TS(76)
#define A_MF(d, a, b, c) "v_mfma_f32_16x16x32_bf16 v[" #d ":" #d "+3], v[" #a ":" #a "+3], v[" #b ":" #b "+3], " c N
#define A_SCORES(W7, W6, W5, W4, W3, W2, W1, W0) \
  W7 A_MF(16, 24, 56, "0") A_MF(20, 28, 56, "0") W6 A_MF(16, 32, 60, "v[16:19]") A_MF(20, 36, 60, "v[20:23]") \
  W5 A_MF(16, 40, 64, "v[16:19]") A_MF(20, 44, 64, "v[20:23]") W4 A_MF(16, 48, 68, "v[16:19]") A_MF(20, 52, 68, "v[20:23]") \
  W3 A_MF(88, 72, 56, "0") W2 A_MF(88, 76, 60, "v[88:91]") W1 A_MF(88, 80, 64, "v[88:91]") W0 A_MF(88, 84, 68, "v[88:91]") \
  "v_cndmask_b32 v16, v16, v20, s[48:49]" N "v_cndmask_b32 v17, v17, v21, s[48:49]" N "v_cndmask_b32 v18, v18, v22, s[48:49]" N "v_cndmask_b32 v19, v19, v23, s[48:49]" N
#define A_RED(op) "s_nop 1" N op "_dpp v92, v92, v92 row_ror:8 row_mask:0xf bank_mask:0xf" N "v_mov_b32 v93, v92" N "s_nop 1" N "v_permlane16_swap_b32 v92, v93" N op " v92, v92, v93" N \
  "v_mov_b32 v93, v92" N "s_nop 1" N "v_permlane32_swap_b32 v92, v93" N op " v92, v92, v93" N
#define A_SOFTMAX "v_max3_f32 v92, v16, v17, v18" N "v_max_f32 v92, v92, v19" N "s_nop 1" N "v_max3_f32 v92, v92, v88, v89" N A_RED("v_max_f32") \
  "v_mul_f32 v92, 0xbe0293ee, v92" N "v_fmamk_f32 v88, v88, 0x3e0293ee, v92" N "v_fmamk_f32 v89, v89, 0x3e0293ee, v92" N "v_fmamk_f32 v16, v16, 0x3e0293ee, v92" N \
  "v_fmamk_f32 v17, v17, 0x3e0293ee, v92" N "v_fmamk_f32 v18, v18, 0x3e0293ee, v92" N "v_fmamk_f32 v19, v19, 0x3e0293ee, v92" N \
  "v_exp_f32 v88, v88" N "v_exp_f32 v89, v89" N "v_exp_f32 v16, v16" N "v_exp_f32 v17, v17" N "v_exp_f32 v18, v18" N "v_exp_f32 v19, v19" N \
  "v_add_f32 v92, v88, v89" N "v_add_f32 v93, v16, v17" N "v_fmac_f32 v93, 0.5, v92" N "v_add_f32 v92, v18, v19" N "v_add_f32 v92, v92, v93" N A_RED("v_add_f32") \
  "v_rcp_f32 v92, v92" N "s_nop 0" N "v_mul_f32 v88, v88, v92" N "v_mul_f32 v89, v89, v92" N "v_mul_f32 v16, v16, v92" N "v_mul_f32 v17, v17, v92" N "v_mul_f32 v18, v18, v92" N "v_mul_f32 v19, v19, v92" N
#define A_NEXT(RETR) A_STAT(24) "s_add_u32 s40, s40, 1" N "s_cmp_lt_u32 s40, 64" N "s_cbranch_scc1 10b" N "s_branch 99f" N A_RETRY(RETR) "99:" N
#define A_FLAG(SO) "s_mov_b64 exec, 1" N "buffer_store_dword %[lim], off, %[rs], " SO " sc1" N "s_mov_b64 exec, -1" N
#define QK_OPS : [kxa] "+&v"(kxa), [qa] "+&v"(qa) \
  : [gv] "v"(gv), [g2] "v"(g2), [la] "v"(la), [wr] "v"(wr_l), [idx] "v"(idx), [kma] "v"(km_l), [pxl] "v"(px_l), [pml] "v"(pm_l), [lim] "v"(lim), \
    "{v[96:127]}"(w0), "{v[128:159]}"(w1), "{v[160:191]}"(w2), "{v[192:223]}"(w3), "{v[224:255]}"(w4), \
    [rs] "s"(rs), [wk] "s"(wk), [o1] "s"(o1), [o2] "s"(o2), [o3] "s"(o3), [tt] "n"(TA + TB), [dv] "n"(DQ), [own] "s"(own), [mw] "s"(mw), [t2] "n"(T2), [tq] "n"(TQ), [th] "n"(TH), [tp] "n"(TP), [dbg] "s"(dbg) \
  : CLOB_V16_95, CLOB_S
constexpr int OOB = 0x40000000;

DEV void role_qk(const Args& a, int h) {
  const int tid = threadIdx.x, lane = tid & 63, w = __builtin_amdgcn_readfirstlane(tid >> 6);
  const u32 tz0 = rt32();
  const rsrc_t rb = __builtin_amdgcn_make_buffer_rsrc(a.ws, 0, 0x0fc00000, 0x00020000);
  v8u W[2][10];
  {
    x_issue(mkrs(a.x, 2048 * 1280), w, x_lane(w, lane), 0, LDS_X0);
    const v4u m0 = mem0_load(a, tid), m1 = mem0_load(a, 512 + (tid & 127));
    v4u o0, o1;
    load_qk(a, h, w, lane, W, o0, o1);
    BAR();
    const int ov = LDS_TX + (w >> 2) * 8192 + (4 * (w & 3) + (lane >> 4)) * 512 + (lane & 15) * 16;
    *(LAS v4u*)(size_t)ov = o0; *(LAS v4u*)(size_t)(ov + 256) = o1;
    mem0_put(m0, tid); mem0_put(m1, 512 + (tid & 127));
  }
  MARK(1);
  const v32u w0 = cat32(W[0][0], W[0][1], W[0][2], W[0][3]), w1 = cat32(W[0][4], W[0][5], W[0][6], W[0][7]), w2 = cat32(W[0][8], W[0][9], W[1][0], W[1][1]),
             w3 = cat32(W[1][2], W[1][3], W[1][4], W[1][5]), w4 = cat32(W[1][6], W[1][7], W[1][8], W[1][9]);
  const PM pm = get_pol(a);
  const int pol = near13(pm, 0);
  const v4i rs = mkrs(a.ws, 0x0fc00000);
  const int u = w < 4 ? w - 1 : 3;
  const int wk = UNI(pol), mw = UNI(w * 1024), own = UNI(OFF_CT + h * 128), dbg = UNI(OFF_DBG + (h * 8 + w) * 2048);
#define QK_LANE int ln = threadIdx.x & 63; PINV(ln); \
  const int g = ln >> 4, i16 = ln & 15, hf = (ln >> 3) & 1, row = ln & 7, idx = hf ? 0xEEEE : 0x4444, gv = ln * 16 + w * 1024, la = ln * 16; \
  const int wr_l = LDS_TM + (w >> 2) * 2048 + (4 * (w & 3) + (i16 >> 2)) * 128 + (4 * (g & 1) + 2 * (g >> 1)) * 16 + (ln & 3) * 4; \
  const int km_l = LDS_TM + 2048 + g * 128 + (2 * (i16 >> 2) + (i16 & 1)) * 16; \
  int kxa = LDS_TX + 8192 + g * 512 + i16 * 16; (void)row;
  if (w == 0) {
    QK_LANE
    int qa = LDS_TM + g * 128 + row * 16;
    const int px_l = !(g & 1) ? ((h >> 4) << 14) + ((h >> 1) & 7) * 1024 + (4 * (h & 1) + g + hf) * 128 + row * 16 + pol
                   : ln < 24 ? 32768 + (h >> 3) * 1024 + (h & 7) * 128 + row * 16 + pol : OOB;
    const int pm_l = la, lim = la, g2 = ln * 4, o1 = 0, o2 = 0, o3 = 0;
    gate(a, tz0 + G1, D0);
    asm volatile(A_INIT "s_setprio 3" N "s_mov_b32 s40, 0" N "s_mov_b32 s47, 0xEEEE" N "v_cmp_eq_u32 s[48:49], s47, %[idx]" N
      QK_CORE("s_mul_i32 s43, s47, " STR(P_SLOT) N "s_add_u32 s43, s43, " STR(OFF_P) N "s_xor_b32 s47, s47, 2" N "s_mul_i32 s44, s47, " STR(P_SLOT) N "s_add_u32 s44, s44, " STR(OFF_P) N A_ASET, "", "", A_KXREAD A_W1, "", A_ANN0, A_ANN1, "")
      "s_cbranch_scc1 15f" N
      ".set ii,0" N ".rept 4" N "ds_read_b128 v[56+4*ii:59+4*ii], %[qa] offset:512*ii" N ".set ii,ii+1" N ".endr" N
      ".set ii,0" N ".rept 4" N "ds_read_b128 v[72+4*ii:75+4*ii], %[kma] offset:512*ii" N ".set ii,ii+1" N ".endr" N
      A_SCORES(A_LW(7), A_LW(6), A_LW(5), A_LW(4), A_LW(3), A_LW(2), A_LW(1), A_LW(0)) A_SOFTMAX
      "v_cvt_pk_bf16_f32 v22, |v88|, |v89|" N "v_cvt_pk_bf16_f32 v23, |v88|, |v89|" N "v_cvt_pk_bf16_f32 v20, |v16|, |v17|" N "v_cvt_pk_bf16_f32 v21, |v18|, |v19|" N
      "v_permlane32_swap_b32 v22, v23" N "s_nop 1" N "v_permlane16_swap_b32 v20, v22" N "v_permlane16_swap_b32 v21, v23" N
      "buffer_store_dwordx4 v[20:23], %[pxl], %[rs], s43 offen sc1" N A_TS(78)
      "v_mov_b64 v[16:17], -1" N "v_mov_b64 v[18:19], -1" N
      "buffer_store_dwordx4 v[16:19], %[pxl], %[rs], s44 offen sc1" N
      "v_xor_b32 %[kxa], 49152, %[kxa]" N
      A_ANN2 "15:" N A_NEXT(A_RETR) QK_OPS);
  } else if (w < 4 || w == 5) {
    QK_LANE
    int qa = LDS_TX + g * 512 + (8 * u + row) * 16;
    const int px_l = hf * OOB + (h >> 2) * 32768 + ((h & 3) * 2 + (u >> 1)) * 1024 + (g * 16 + (u & 1) * 8 + row) * 16, g2 = la;
    const int pm_l = min(ln >> 3, 1) * OOB + (8 + (h >> 4)) * 32768 + (((h >> 2) & 3) * 2 + (u >> 1)) * 1024 + ((h & 3) * 16 + (u & 1) * 8 + row) * 16;
    const int lim = 8 * u + row - (16 * hf + 4 * g);
    const int o1 = UNI((int)(pm >> 40)), o2 = UNI(OFF_FP + (h * 4 + u) * 4 - 512), o3 = UNI(w == 5);
    { const v4u on = {~0u, ~0u, ~0u, ~0u}; ST128(on, px_l, pt_off(62, pm)); ST128(on, pm_l, pt_off(62, pm)); ST128(on, px_l, pt_off(63, pm)); ST128(on, pm_l, pt_off(63, pm)); }
    asm volatile(A_INIT "s_mov_b32 s40, 0" N "s_mov_b32 s47, 0xEEEE" N "v_cmp_eq_u32 s[48:49], s47, %[idx]" N
      "v_cmp_le_i32 s[50:51], 0, %[lim]" N "v_cmp_le_i32 s[52:53], 1, %[lim]" N "v_cmp_le_i32 s[54:55], 2, %[lim]" N "v_cmp_le_i32 s[56:57], 3, %[lim]" N
      "s_mov_b32 s44, %[o2]" N "s_mov_b32 s64, " STR(OFF_CTRL) "+65536" N "s_mov_b32 s65, 0" N "s_mov_b32 s69, %[o1]" N
      QK_CORE("s_and_b32 s43, s69, 1" N "s_lshl_b32 s43, 0x2000, s43" N "s_add_u32 s43, s43, s64" N, "", "", "s_cmp_lg_u32 %[o3], 0" N "s_cbranch_scc1 51f" N A_WV "51:" N A_WL, A_WV, "", "", "")
      "s_cmp_eq_u32 s40, 0" N "s_cbranch_scc1 14f" N A_FLAG("s44") "14:" N "s_cmp_eq_u32 s40, 63" N "s_cbranch_scc1 52f" N "s_sleep 2" N "52:" N
      ".set ii,0" N ".rept 4" N "ds_read_b128 v[56+4*ii:59+4*ii], %[qa] offset:2048*ii" N ".set ii,ii+1" N ".endr" N
      A_KXREAD "s_waitcnt lgkmcnt(0)" N
      ".set ii,0" N ".rept 4" N "ds_read_b128 v[72+4*ii:75+4*ii], %[kma] offset:512*ii" N ".set ii,ii+1" N ".endr" N
      "v_mov_b32 v94, 0xd02877d7" N
      A_SCORES("", "", "", "", A_LW(3), A_LW(2), A_LW(1), A_LW(0))
      "v_cndmask_b32 v16, v94, v16, s[50:51]" N "v_cndmask_b32 v17, v94, v17, s[52:53]" N "v_cndmask_b32 v18, v94, v18, s[54:55]" N "v_cndmask_b32 v19, v94, v19, s[56:57]" N
      A_SOFTMAX
      "v_cvt_pk_bf16_f32 v20, |v16|, |v17|" N "v_cvt_pk_bf16_f32 v22, |v18|, |v19|" N "v_cvt_pk_bf16_f32 v24, |v88|, |v89|" N "v_cvt_pk_bf16_f32 v25, |v88|, |v89|" N
      "v_mov_b32_dpp v21, v20 row_ror:8 row_mask:0xf bank_mask:0xf" N "v_mov_b32_dpp v23, v22 row_ror:8 row_mask:0xf bank_mask:0xf" N
      "v_permlane16_swap_b32 v24, v25" N
      "buffer_store_dwordx4 v[20:23], %[pxl], %[rs], s43 offen sc1" N
      "v_mov_b32 v26, v24" N "v_mov_b32 v27, v25" N "s_nop 1" N "v_permlane32_swap_b32 v24, v26" N "v_permlane32_swap_b32 v25, v27" N
      "buffer_store_dwordx4 v[24:27], %[pml], %[rs], s43 offen sc1" N A_TS(78)
      "v_xor_b32 %[kxa], 49152, %[kxa]" N "v_xor_b32 %[qa], 49152, %[qa]" N "s_add_u32 s44, s44, 512" N
      "s_add_u32 s64, s64, 327680" N "s_add_u32 s65, s65, 1" N "s_cmp_lt_u32 s65, 6" N "s_cbranch_scc1 53f" N "s_mov_b32 s65, 0" N "s_add_u32 s64, s64, 131072" N "s_lshr_b32 s69, s69, 1" N "53:" N
      A_NEXT("") "s_waitcnt vmcnt(0)" N A_FLAG("s44") QK_OPS);
  } else if (w == 4) {
    QK_LANE
    int qa = 0;
    const int px_l = la, pm_l = la, lim = la, g2 = la, o1 = 0, o2 = 0, o3 = 0;
    asm volatile(A_INIT "s_mov_b32 s40, 0" N QK_CORE(A_TSET, A_TIMER, "", A_WL, A_WV, "", "", "") A_NEXT("") QK_OPS);
  } else {
    QK_LANE
    int qa = -1;
    const int px_l = la, pm_l = la, lim = (qkx_off(ln, h, pm) ^ 8192) + (w & 1) * 16384, g2 = la;
    const int o1 = UNI(OFF_FQ + h * 4), o2 = 0, o3 = UNI(LDS_TX + (w & 1) * 8192);
#define EARLY "63"
#define QK_TX ".set ii,0" N ".rept 2" N "s_add_u32 m0, s63, 4096*ii" N "s_add_u32 s47, s44, 4096*ii" N \
      "buffer_load_dwordx4 %[la], %[rs], s47 offen sc1 lds" N "buffer_load_dwordx4 %[la], %[rs], s47 offen offset:1024 sc1 lds" N \
      "buffer_load_dwordx4 %[la], %[rs], s47 offen offset:2048 sc1 lds" N "buffer_load_dwordx4 %[la], %[rs], s47 offen offset:3072 sc1 lds" N ".set ii,ii+1" N ".endr" N
    asm volatile(A_INIT "s_mov_b32 s40, -1" N "s_mov_b32 s43, %[o1]" N "s_mov_b32 s63, %[o3]" N "s_mov_b32 s69, 0" N "s_branch 17f" N
      QK_CORE(A_SLOT2 "s_waitcnt vmcnt(0)" N "v_cmp_ne_u32 vcc, -1, %[qa]" N "s_cmp_lt_u32 s40, " EARLY N "s_cselect_b32 s69, vcc_lo, 0" N, "", A_DMA2,
        A_WL, A_WV, "", "", "s_cmp_eq_u32 s69, -1" N "s_cbranch_scc0 41f" N QK_TX "s_mov_b32 s69, 1" N "41:" N)
      "s_cbranch_scc1 15f" N
      "16:" N "s_cmp_lg_u32 s69, 0" N "s_cbranch_scc1 17f" N A_POLL(21, 22, "%[qa]", "off", "s43") QK_TX
      "17:" N "s_add_u32 s47, s40, 2" N "s_xor_b32 s63, s63, 49152" N "s_add_u32 s43, s43, 128" N "v_readlane_b32 s44, %[lim], s47" N
      "buffer_load_dword %[qa], off, %[rs], s43 sc1" N
      "15:" N A_NEXT("") QK_OPS);
  }
}

#define A_SO(d, u, b) "v_smfmac_f32_16x16x64_bf16 v[" #d ":" #d "+3], v[56+4*" #u ":59+4*" #u "], v[" #b ":" #b "+7], %[idx]" N
#define A_CM(d, a) "v_cvt_pk_bf16_f32 v" #d ", v[" #a "], v[" #a "+1]" N "v_min_u32 v" #d ", 0xfffeffff, v" #d N
#define VO_P3A A_SO(28, 0, 240) A_SO(28, 1, 248) A_SO(28, 2, 40) A_SO(28, 3, 48)
#define VO_P3B A_CM(36, 28) A_CM(37, 30) "buffer_store_dwordx2 v[36:37], %[zml2], %[rs], s64 offen sc1" N
#define VO_P3C "buffer_store_dwordx2 v[92:93], %[zml2], %[rs], s65 offen sc1" N
#define VO_CORE(SETUP, TIMER, DMA2, P3A, P3B, P3C, ANN0, ANN1, W0, W0B, RETR) \
  A_INIT "s_mov_b32 s40, 0" N \
  "10:" N A_SLOT SETUP "s_add_u32 s47, s40, 2" N "v_readlane_b32 s64, %[tab], s40" N "v_readlane_b32 s65, %[tab], s47" N \
  ".set ii,0" N ".rept 4" N "v_mov_b64 v[24+2*ii:25+2*ii], 0" N ".set ii,ii+1" N ".endr" N \
  A_ZERO "s_cmp_eq_u32 s40, 0" N "s_cbranch_scc1 20f" N TIMER "11:" N A_GATHER(DMA2, W0) \
  A_LW(3) A_S0(0) A_LW(2) A_S0(1) A_GATHER2(W0B, ANN0) A_LW(7) A_S0(2) A_LW(6) A_S0(3) A_LW(5) ANN1 A_S0(4) A_LW(4) A_S0(5) A_LW(3) A_S0(6) A_LW(2) A_S0(7) A_LW(1) A_S0(8) A_LW(0) A_S0(9) \
  "s_nop 7" N \
  "v_permlane32_swap_b32 v16, v18" N "v_permlane32_swap_b32 v17, v19" N "v_pk_add_f32 v[16:17], v[16:17], v[18:19]" N "s_nop 0" N "v_cmp_u_f32 vcc, v16, v17" N "v_cvt_pk_bf16_f32 v16, v16, v17" N \
  "ds_write_b16 %[wr], v16" N "ds_write_b16_d16_hi %[wr], v16 offset:16" N "s_cbranch_vccnz 19f" N "13:" N A_TSB "s_waitcnt lgkmcnt(0)" N "s_barrier" N A_TS(76) \
  ".set ii,0" N ".rept 4" N "ds_read_b128 v[56+4*ii:59+4*ii], %[va] offset:512*ii" N ".set ii,ii+1" N ".endr" N \
  A_LW(3) A_SO(20, 0, 176) A_SO(24, 0, 208) A_LW(2) A_SO(20, 1, 184) A_SO(24, 1, 216) A_LW(1) A_SO(20, 2, 192) A_SO(24, 2, 224) A_LW(0) A_SO(20, 3, 200) A_SO(24, 3, 232) \
  P3A A_CM(32, 20) A_CM(33, 22) A_CM(34, 24) A_CM(35, 26) "s_nop 1" N \
  "v_permlane16_swap_b32 v32, v34" N "v_permlane16_swap_b32 v33, v35" N \
  "buffer_store_dwordx4 v[32:35], %[zml], %[rs], s64 offen sc1" N P3B A_TS(78) \
  "s_cmp_gt_u32 s40, 61" N "s_cbranch_scc1 15f" N "v_mov_b64 v[92:93], -1" N "v_mov_b64 v[94:95], -1" N \
  "buffer_store_dwordx4 v[92:95], %[zml], %[rs], s65 offen sc1" N P3C \
  "15:" N A_NEXT(RETR)
#define VO_OPS : : [gv] "v"(gv), [g2] "v"(g2), [la] "v"(la), [wr] "v"(wr_l), [idx] "v"(idx), [zml] "v"(zm_l), [zml2] "v"(zm_l2), [va] "v"(va), [tab] "v"(tab), \
    "{v[96:127]}"(w0), "{v[128:159]}"(w1), "{v[160:191]}"(w2), "{v[192:223]}"(w3), "{v[224:255]}"(w4), "{v[40:55]}"(w5), \
    [rs] "s"(rs), [wk] "s"(wk), [tt] "n"(TA + TB), [dv] "n"(DV), [own] "s"(own), [mw] "s"(mw), [t2] "n"(T2), [tq] "n"(TQ), [th] "n"(TH), [tp] "n"(TP), [dbg] "s"(dbg) \
  : "v14", "v15", "v16", "v17", "v18", "v19", V10(2), V10(3), "v56", "v57", "v58", "v59", V10(6), V10(7), V10(8), "v90", "v91", "v92", "v93", "v94", "v95", CLOB_S

DEV void role_vo(const Args& a, int h) {
  const int tid = threadIdx.x, lane = tid & 63, w = __builtin_amdgcn_readfirstlane(tid >> 6);
  const u32 tz0 = rt32();
  const rsrc_t rb = __builtin_amdgcn_make_buffer_rsrc(a.ws, 0, 0x0fc00000, 0x00020000);
  v8u Wv[10], Wo[3][4];
  const int t0 = w < 4 ? 6 * w : 8 + 4 * w;
  {
    const v4i rv = mkrs(a.wv, 640 * 8192), ro = mkrs(a.wo, 4096 * 1280);
    const StA st = stA_init(w, lane, h);
    const StB sb = stB_init(w, lane, h);
    const int b0 = stA_base(lane, 2 * w), r0 = 64 * (w >> 2);
    const v4u m0 = mem0_load(a, tid), m1 = mem0_load(a, 512 + (tid & 127));
    stA_issue(st, rv, r0, 0); stA_issue(st, rv, 128 + r0, STAGE_BUF);
#pragma unroll
    for (int r = 0; r < 5; r++) {
      if (r < 4) STAGE_WAIT(4); else STAGE_WAIT(5);
      Wv[2 * r] = stA_read(b0, (r & 1) * STAGE_BUF);
      Wv[2 * r + 1] = stA_read(b0, (r & 1) * STAGE_BUF + 16384);
      BAR();
      if (r + 2 < 5) stA_issue(st, rv, 128 * (r + 2) + r0, (r & 1) * STAGE_BUF); else stB_issue(sb, ro, w, r - 3, r & 1);
    }
    const int q = (lane >> 2) & 3, row = 4 * (lane >> 4) + (q & 1);
#pragma unroll
    for (int r = 0; r < 4; r++) {
      if (r < 3) STAGE_WAIT(5); else STAGE_WAIT(0);
#pragma unroll
      for (int t = 0; t < 3; t++) {
        const int c0 = 2 * min(t0 + 2 * t + (q >> 1), 39);
        const int a0 = stB_addr(row, c0, lane) + ((r + 1) & 1) * STAGE_BUF, a1 = stB_addr(row + 16, c0, lane) + ((r + 1) & 1) * STAGE_BUF;
        Wo[t][r] = cat8(cat4(ldstr(a0), ldstr(a0 + 2560)), cat4(ldstr(a1), ldstr(a1 + 2560)));
      }
      BAR();
      if (r + 2 < 4) stB_issue(sb, ro, w, r + 2, (r + 1) & 1);
    }
    mem0_put(m0, tid); mem0_put(m1, 512 + (tid & 127));
  }
  MARK(1);
  const v32u w0 = cat32(Wv[0], Wv[1], Wv[2], Wv[3]), w1 = cat32(Wv[4], Wv[5], Wv[6], Wv[7]), w2 = cat32(Wv[8], Wv[9], Wo[0][0], Wo[0][1]),
             w3 = cat32(Wo[0][2], Wo[0][3], Wo[1][0], Wo[1][1]), w4 = cat32(Wo[1][2], Wo[1][3], Wo[2][0], Wo[2][1]);
  const v16u w5 = __builtin_shufflevector(Wo[2][2], Wo[2][3], 0, 1, 2, 3, 4, 5, 6, 7, 8, 9, 10, 11, 12, 13, 14, 15);
  const PM pm = get_pol(a);
  const int pol = near13(pm, 0);
  const v4i rs = mkrs(a.ws, 0x0fc00000);
  const int wk = UNI(pol), mw = UNI(w * 1024), own = UNI(OFF_CT + 4096 + h * 128), dbg = UNI(OFF_DBG + ((32 + h) * 8 + w) * 2048);
#define VO_LANE int ln = threadIdx.x & 63; PINV(ln); \
  const int g = ln >> 4, n = ln & 15, idx = (ln & 8) ? 0xEEEE : 0x4444, gv = ln * 16 + w * 1024, g2 = ln * 4, la = ln * 16; \
  const int wr_l = LDS_VM + w * 256 + (n >> 3) * 128 + (4 * (g & 1) + 2 * (g >> 1)) * 16 + (n & 7) * 2, va = LDS_VM + g * 128 + (ln & 7) * 16; \
  const int zm_l = (t0 + (g & 1) * 2 + (g >> 1)) * 16384 + h * 256 + n * 16, zm_l2 = (t0 + 4 + (g >> 1)) * 16384 + h * 256 + n * 16 + (g & 1) * 8, tab = zm_off(ln, pm);
  {
    VO_LANE
    const v4u on = {~0u, ~0u, ~0u, ~0u};
    for (int k = 0; k < 2; k++) {
      const int so = zm_off(k, pm);
      ST128(on, zm_l, so);
      if (w < 4) ST64(((v2u){~0u, ~0u}), zm_l2, so);
    }
    (void)idx; (void)g2; (void)gv; (void)la; (void)wr_l; (void)va; (void)tab;
  }
  if (w == 0) gate(a, tz0 + G1, D0);
  if (w == 0) { VO_LANE asm volatile(VO_CORE(A_ASET, "", "", VO_P3A, VO_P3B A_ANN2, VO_P3C, A_ANN0, A_ANN1, A_W1, "", A_RETR) VO_OPS); }
  else if (w < 4) { VO_LANE asm volatile(VO_CORE("", "", "", VO_P3A, VO_P3B, VO_P3C, "", "", A_W0, "", "") VO_OPS); }
  else if (w == 4) { VO_LANE asm volatile(VO_CORE(A_TSET, A_TIMER, "", "s_nop 6" N, "", "", "", "", A_WL, A_WV, "") VO_OPS); }
  else if (w == 5) { VO_LANE asm volatile(VO_CORE("", "", "", "s_nop 6" N, "", "", "", "", A_WL, A_WV, "") VO_OPS); }
  else { VO_LANE asm volatile(VO_CORE(A_SLOT2, "", A_DMA2, "s_nop 6" N, "", "", "", "", A_WL, A_WV, "") VO_OPS); }
}

#define Z_LD4(r, V, SO) "buffer_load_dwordx4 v[" #r ":" #r "+3], " V ", %[rs], " SO " offen sc1" N "buffer_load_dwordx4 v[" #r "+4:" #r "+7], " V ", %[rs], " SO " offen offset:1024 sc1" N \
  "buffer_load_dwordx4 v[" #r "+8:" #r "+11], " V ", %[rs], " SO " offen offset:2048 sc1" N "buffer_load_dwordx4 v[" #r "+12:" #r "+15], " V ", %[rs], " SO " offen offset:3072 sc1" N
#define Z_SPEC
#define Z_C0
#define Z_C1
#define Z_CLR
#define Z_SLACK
#define Z_LATEST A_LATEST("v255", "s62", "%[tb]") A_PEN("s62", "%[tb]", "s65") "s_add_u32 s63, s62, s47" N "s_add_u32 s63, s63, %[ta]" N
#define Z_LITE
#define ZH 4
DEV void role_z(const Args& a, int zc) {
  const int tid = threadIdx.x, lane = tid & 63;
  const u32 tz0 = rt32();
  calibrate(a, zc);
  const int wz = UNI(tid >> 6);
  if (wz > ZH) return;
  MARK(1);
  const PM pm = get_pol(a);
  const int pol = near13(pm, 0), ztab = zm_off(lane, pm) + zc * 16384;
  const v4i rs = mkrs(a.ws, 0x0fc00000);
  if (wz > 0) {
    const rsrc_t rb = __builtin_amdgcn_make_buffer_rsrc(a.ws, 0, 0x0fc00000, 0x00020000);
    const int q = wz - 1, fo = (q * 8 + (lane & 7)) * 4, alt = zc % 10 < 5 ? 0 : OFF_FY - OFF_FZ, xtab = zx_off(lane, zc, 8 * q);
    int budget = BUDGET;
    u32 f = ~0u;
    wait_until(tz0 + ZS);
#pragma unroll 1
    for (int s = 0; s < 63; s++) {
      TS(h0);
      TS(h1);
      if (s == 1) asm volatile("s_barrier" ::: "memory");
      const int so = __builtin_amdgcn_readlane(xtab, s), lb = (s & 1) * 32768 + q * 8192;
      for (u32 pend = 0xFF;;) {
        u32 rdy = (u32)__builtin_amdgcn_ballot_w64(f != 0xFFFFFFFFu) & pend;
        pend &= ~rdy;
        for (; rdy; rdy &= rdy - 1) { const int i = __builtin_ctz(rdy); dmac(rs, lb + i * 1024, lane * 16, so + i * 1024); }
        if (!pend || --budget < 0) break;
        __builtin_amdgcn_s_sleep(GSL);
        f = LD32(fo, OFF_FZ + s * 128 + (s < 2 ? alt : 0));
      }
      f = LD32(fo, OFF_FZ + s * 128 + 128 + (s < 1 ? alt : 0));
      u32 t = LD32(0, __builtin_amdgcn_readlane(ztab, s) ^ 8192);
      PINV(t);
      asm volatile("s_waitcnt vmcnt(0)\n\ts_barrier" ::: "memory");
    }
    if (zc == 0 && tid == 64) ST64(mark(LD64(0, OFF_E)), 0, OFF_E);
    return;
  }
  const int g = lane >> 4, n = lane & 15;
  const int idx = (lane & 8) ? 0xEEEE : 0x4444;
  const int la = lane * 16;
  const int lo = zc * 256 + ((n >> 1) & 1) * 128 + (4 * (g & 1) + 2 * (g >> 1) + (n & 1)) * 16;
  const int pub_l = n < 4 ? ((lo >> 13) << 14) + (lo & 8191) + pol : OOB;
  const u32 sel = (n & 1) ? 0x03020706u : 0x05040100u;
  int lx = la;
  const int cb = UNI(OFF_CT + pol), pb = UNI(OFF_P + pol);
  const int dbg = OFF_DBG + (64 + zc) * 8 * 2048;
  asm volatile(A_INIT "s_mov_b32 s40, 0" N "s_memrealtime s[58:59]" N "s_waitcnt lgkmcnt(0)" N "s_mov_b32 s63, s58" N "s_mov_b32 s64, 0" N
    "10:" N Z_CLR "v_readlane_b32 s42, %[ztab], s40" N "s_mov_b32 s66, 0" N A_CT(s61, "%[cb]") "s_barrier" N
    ".set ii,0" N ".rept 32" N "ds_read_b128 v[10+4*ii:13+4*ii], %[lx] offset:1024*ii" N ".set ii,ii+1" N ".endr" N
    "v_xor_b32 %[lx], 32768, %[lx]" N
    "s_and_b32 s47, s40, 3" N "s_mul_i32 s50, s47, " STR(P_SLOT) N "s_add_u32 s50, s50, %[pb]" N "s_add_u32 s51, s50, 4096" N "s_add_u32 s52, s50, 16384" N "s_add_u32 s53, s50, 20480" N "s_add_u32 s54, s50, 32768" N
    "s_add_u32 s47, s40, 1" N "s_and_b32 s47, s47, 3" N "s_lshl_b32 s55, s47, 15" N "s_xor_b32 s56, s55, 65536" N "s_add_u32 s57, s42, 4096" N
    "s_add_u32 s60, s63, %[tc]" N "s_mov_b32 s65, s63" N "s_add_u32 s62, s63, %[tl]" N "s_sub_u32 s67, s62, %[td]" N "s_cmp_eq_u32 s40, 0" N "s_cselect_b32 s47, 0x10000000, -2" N "s_add_u32 s67, s67, s47" N "s_mov_b32 s68, 0" N
    "24:" N "v_lshlrev_b32 v251, 3, %[la]" N "v_mov_b32 v255, -2" N A_WAIT(35, 36) Z_C0 "buffer_load_dword v255, v251, %[rs], s61 offen sc1" N "s_add_u32 s60, s60, %[tq]" N A_FAIL(s80)
    "25:" N "s_memrealtime s[58:59]" N "v_cmp_eq_u32 vcc, -2, v255" N "s_cbranch_vccz 28f" N "s_waitcnt lgkmcnt(0)" N "s_sub_u32 s47, s67, s58" N "s_cmp_gt_i32 s47, 0" N "s_cbranch_scc1 25b" N
    "s_mov_b32 s68, 1" N Z_SPEC "s_branch 27f" N
    "28:" N Z_C1 "s_add_u32 s67, s60, 0x10000000" N "v_cmp_eq_u32 vcc, -1, v255" N "s_cbranch_vccz 26f" N
    "s_sub_u32 s45, s45, 1" N "s_cmp_lt_i32 s45, 0" N "s_cbranch_scc0 24b" N
    "26:" N Z_LATEST Z_SLACK
    "27:" N "s_sub_u32 s60, s62, %[td]" N A_TS(74) A_WAIT(31, 33)
    Z_LD4(138, "%[la]", "s42") Z_LD4(154, "%[la]", "s57")
    "s_mov_b32 s60, s62" N "v_mov_b64 v[250:251], 0" N "v_mov_b64 v[252:253], 0" N A_WAIT(32, 34)
    A_TS(72)
    Z_LD4(170, "%[la]", "s50") Z_LD4(186, "%[la]", "s51") Z_LD4(202, "%[la]", "s52") Z_LD4(218, "%[la]", "s53") Z_LD4(234, "%[la]", "s54")
    "s_waitcnt vmcnt(20)" N
    "s_cmp_eq_u32 s68, 0" N "s_cbranch_scc1 29f" N "v_cmp_eq_u32 vcc, -1, v255" N "s_cbranch_vccnz 18f" N
    "v_subrev_u32 v254, s65, v255" N "v_cmp_lt_i32 vcc, %[xx], v254" N "s_cbranch_vccz 29f" N "s_mov_b32 s68, 0" N "s_branch 26b" N "29:" N
    ".set ii,0" N ".rept 16" N "v_permlane16_swap_b32 v[138+2*ii], v[139+2*ii]" N ".set ii,ii+1" N ".endr" N
    ".set ii,0" N ".rept 20" N "s_waitcnt vmcnt(19-ii)" N
    "v_smfmac_f32_16x16x64_bf16 v[250:253], v[170+4*ii:173+4*ii], v[10+8*ii:17+8*ii], %[idx]" N ".set ii,ii+1" N ".endr" N
    "s_nop 7" N
    "v_permlane32_swap_b32 v250, v252" N "v_permlane32_swap_b32 v251, v253" N "v_pk_add_f32 v[250:251], v[250:251], v[252:253]" N "s_nop 0" N
    "v_cvt_pk_bf16_f32 v254, v250, v251" N "v_cmp_u_f32 vcc, v250, v251" N "s_cbranch_vccnz 19f" N
    "13:" N "v_mov_b32_dpp v251, v254 quad_perm:[1,0,3,2] row_mask:0xf bank_mask:0xf" N "v_perm_b32 v250, v251, v254, %[sel]" N "v_min_u32 v250, 0xfffeffff, v250" N "s_nop 1" N
    "v_mov_b32_dpp v251, v250 row_shl:4 row_mask:0xf bank_mask:0xf" N "v_mov_b32_dpp v252, v250 row_shl:8 row_mask:0xf bank_mask:0xf" N "v_mov_b32_dpp v253, v250 row_shl:12 row_mask:0xf bank_mask:0xf" N
    "buffer_store_dwordx4 v[250:253], %[pub], %[rs], s55 offen sc1" N A_TS(78)
    "v_mov_b64 v[246:247], -1" N "v_mov_b64 v[248:249], -1" N "buffer_store_dwordx4 v[246:249], %[pub], %[rs], s56 offen sc1" N
    "s_cmp_eq_u32 s68, 0" N "s_cbranch_scc1 30f" N Z_LATEST "30:" N
    "s_cmp_eq_u32 s40, 0" N "s_cbranch_scc0 14f" N "s_barrier" N "14:" N
    Z_LITE A_STAT(170)
    "s_add_u32 s40, s40, 1" N "s_cmp_lt_u32 s40, 63" N "s_cbranch_scc1 10b" N "s_branch 99f" N
    "19:" N "s_lshl_b32 s66, s66, 1" N "s_or_b32 s66, s66, 1" N "s_cmp_lt_u32 s66, 4" N "s_cbranch_scc1 18f" N
    "v_mov_b32 v252, 0" N ".set ii,0" N ".rept 56" N "v_max3_u32 v252, v252, v[138+2*ii], v[139+2*ii]" N ".set ii,ii+1" N ".endr" N
    "v_cmp_eq_u32 vcc, -1, v252" N "s_cbranch_vccz 13b" N
    "18:" N A_FAIL(s70) "s_sub_u32 s45, s45, 1" N "s_cmp_lt_i32 s45, 0" N "s_cbranch_scc1 13b" N
    "s_memrealtime s[58:59]" N "s_waitcnt lgkmcnt(0)" N "s_mov_b32 s60, s58" N "s_add_u32 s67, s58, 0x10000000" N "s_mov_b32 s68, 0" N "s_branch 24b" N
    "99:" N
    : [lx] "+&v"(lx)
    : [la] "v"(la), [idx] "v"(idx), [pub] "v"(pub_l), [sel] "v"(sel), [ztab] "v"(ztab),
      [rs] "s"(rs), [tb] "n"(TB), [td] "n"(TD), [tc] "n"(TG), [ta] "n"(TA), [tq] "n"(TZ), [th] "n"(TH), [tp] "n"(TP), [tl] "n"(TB + TL), [xx] "n"(XX + TL), [cb] "s"(cb), [pb] "s"(pb), [dbg] "s"(dbg)
    : V10(1), V10(2), V10(3), V10(4), V10(5), V10(6), V10(7), V10(8), V10(9), V10(10), V10(11), V10(12), V10(13), V10(14), V10(15), V10(16), V10(17), V10(18), V10(19),
      V10(20), V10(21), V10(22), V10(23), V10(24), "v250", "v251", "v252", "v253", "v254", "v255", CLOB_S);
}

#define W_OPS "{v[96:127]}"(w0), "{v[128:159]}"(w1), "{v[160:191]}"(w2), "{v[192:223]}"(w3), "{v[224:255]}"(w4)
#define W_ARGS const v32u& w0, const v32u& w1, const v32u& w2, const v32u& w3, const v32u& w4
#define W_CAT const v32u w0 = cat32(W[0][0], W[0][1], W[0][2], W[0][3]), w1 = cat32(W[0][4], W[0][5], W[0][6], W[0][7]), w2 = cat32(W[0][8], W[0][9], W[1][0], W[1][1]), \
             w3 = cat32(W[1][2], W[1][3], W[1][4], W[1][5]), w4 = cat32(W[1][6], W[1][7], W[1][8], W[1][9])
#define B_MF "v_mfma_f32_16x16x32_bf16 "
#define B_RD "ds_read_b128 v[24+8*(ii&3):27+8*(ii&3)], %[xb] offset:1024*ii" N "ds_read_b128 v[28+8*(ii&3):31+8*(ii&3)], %[xb] offset:20480+1024*ii" N
#define B_ST(c0, c1, c2, c3) B_MF "v[56:59], v[96+4*kk:99+4*kk], v[24+8*(kk&3):27+8*(kk&3)], " c0 N B_MF "v[60:63], v[176+4*kk:179+4*kk], v[24+8*(kk&3):27+8*(kk&3)], " c1 N \
  B_MF "v[64:67], v[96+4*kk:99+4*kk], v[28+8*(kk&3):31+8*(kk&3)], " c2 N B_MF "v[68:71], v[176+4*kk:179+4*kk], v[28+8*(kk&3):31+8*(kk&3)], " c3 N ".set kk,kk+1" N
#define B_STA B_ST("v[56:59]", "v[60:63]", "v[64:67]", "v[68:71]")
DEV void proj_tile(W_ARGS, int xb, v4u& o0, v4u& o1) {
  asm volatile(".set ii,0" N ".set kk,0" N ".rept 4" N B_RD ".set ii,ii+1" N ".endr" N
    A_LW(6) B_ST("0", "0", "0", "0") B_RD ".set ii,ii+1" N
    ".rept 15" N A_LW(6) B_STA B_RD ".set ii,ii+1" N ".endr" N
    A_LW(6) B_STA A_LW(4) B_STA A_LW(2) B_STA A_LW(0) B_STA "s_nop 5" N
    "v_cvt_pk_bf16_f32 v72, v56, v60" N "v_cvt_pk_bf16_f32 v73, v57, v61" N "v_cvt_pk_bf16_f32 v74, v58, v62" N "v_cvt_pk_bf16_f32 v75, v59, v63" N
    "v_cvt_pk_bf16_f32 v76, v64, v68" N "v_cvt_pk_bf16_f32 v77, v65, v69" N "v_cvt_pk_bf16_f32 v78, v66, v70" N "v_cvt_pk_bf16_f32 v79, v67, v71" N
    : "={v[72:75]}"(o0), "={v[76:79]}"(o1) : [xb] "v"(xb), W_OPS
    : "v24", "v25", "v26", "v27", "v28", "v29", V10(3), V10(4), V10(5), V10(6), "v70", "v71", "memory");
}
#define Z_T(c) B_MF "v[" #c ":" #c "+3], v[24:27], v[96+8*tt:99+8*tt], 0" N B_MF "v[" #c "+4:" #c "+7], v[28:31], v[96+8*tt:99+8*tt], 0" N \
  B_MF "v[" #c ":" #c "+3], v[32:35], v[100+8*tt:103+8*tt], v[" #c ":" #c "+3]" N B_MF "v[" #c "+4:" #c "+7], v[36:39], v[100+8*tt:103+8*tt], v[" #c "+4:" #c "+7]" N \
  B_MF "v[" #c ":" #c "+3], v[40:43], v[176+8*tt:179+8*tt], v[" #c ":" #c "+3]" N B_MF "v[" #c "+4:" #c "+7], v[44:47], v[176+8*tt:179+8*tt], v[" #c "+4:" #c "+7]" N \
  B_MF "v[" #c ":" #c "+3], v[48:51], v[180+8*tt:183+8*tt], v[" #c ":" #c "+3]" N B_MF "v[" #c "+4:" #c "+7], v[52:55], v[180+8*tt:183+8*tt], v[" #c "+4:" #c "+7]" N ".set tt,tt+1" N
#define Z_O(c, d) "v_cvt_pk_bf16_f32 v[" #d "], v[" #c "], v[" #c "+1]" N "v_cvt_pk_bf16_f32 v[" #d "+1], v[" #c "+4], v[" #c "+5]" N \
  "v_cvt_pk_bf16_f32 v[" #d "+2], v[" #c "+2], v[" #c "+3]" N "v_cvt_pk_bf16_f32 v[" #d "+3], v[" #c "+6], v[" #c "+7]" N \
  "buffer_store_dwordx4 v[" #d ":" #d "+3], %[la], %[rs], %[so] offen sc1" N "s_add_u32 %[so], %[so], 65536" N
DEV void z_tiles(W_ARGS, int vb, int la, v4i rs, int so) {
  asm volatile(".set ii,0" N ".set tt,0" N ".rept 4" N "ds_read_b128 v[24+8*ii:27+8*ii], %[vb] offset:2048*ii" N "ds_read_b128 v[28+8*ii:31+8*ii], %[vb] offset:2048*ii+256" N ".set ii,ii+1" N ".endr" N
    A_LW(0) Z_T(56) Z_T(64) Z_O(56, 72)
    ".rept 4" N Z_T(56) Z_O(64, 76) Z_T(64) Z_O(56, 72) ".endr" N
    "s_nop 7" N Z_O(64, 76)
    : [so] "+&s"(so) : [vb] "v"(vb), [la] "v"(la), [rs] "s"(rs), W_OPS
    : "v24", "v25", "v26", "v27", "v28", "v29", V10(3), V10(4), V10(5), V10(6), V10(7), "scc", "memory");
}
template <int T0> DEV void z_half(W_ARGS, int vb, int la, v4i rs, int so) {
  asm volatile(".set ii,0" N ".set tt,%c[t0]" N ".rept 4" N "ds_read_b128 v[24+8*ii:27+8*ii], %[vb] offset:2048*ii" N "ds_read_b128 v[28+8*ii:31+8*ii], %[vb] offset:2048*ii+256" N ".set ii,ii+1" N ".endr" N
    A_LW(0) Z_T(64)
    ".rept 2" N Z_T(56) Z_O(64, 76) Z_T(64) Z_O(56, 72) ".endr" N
    "s_nop 7" N Z_O(64, 76)
    : [so] "+&s"(so) : [vb] "v"(vb), [la] "v"(la), [rs] "s"(rs), [t0] "n"(T0), W_OPS
    : "v24", "v25", "v26", "v27", "v28", "v29", V10(3), V10(4), V10(5), V10(6), V10(7), "scc", "memory");
}
#undef N

DEV PM role_qkb(const Args& a, int h, int par) {
  const int tid = threadIdx.x, lane = tid & 63, w = __builtin_amdgcn_readfirstlane(tid >> 6);
  const rsrc_t rb = __builtin_amdgcn_make_buffer_rsrc(a.ws, 0, 0x0fc00000, 0x00020000);
  const v4i rx = mkrs(a.x, 2048 * 1280);
  const int g = lane >> 4, n = lane & 15, xv = x_lane(w, lane);
  x_issue(rx, w, xv, 2 - par, LDS_X0);
  v8u W[2][10];
  v4u o0, o1;
  load_qk(a, h, w, lane, W, o0, o1);
  MARK(1);
  W_CAT;
  const v4u pv = pol_load(a);
  PM pm = 0;
  const int ov = (w >> 2) * 16384 + (4 * (w & 3) + g) * 512 + n * 16;
#pragma unroll 1
  for (int s = 2 - par, i = 0; s < 64; s += 2, i++) {
    if (i < 6) asm volatile("s_waitcnt vmcnt(0)\n\ts_barrier" ::: "memory"); else asm volatile("s_waitcnt vmcnt(2)\n\ts_barrier" ::: "memory");
    if (i == 0) pm = pol_get(a, pv);
    if (i != 0 && i != 6 && tid == 0) ST32(0u, 0, UNI(OFF_FQ + ((i < 6 ? s - 2 : s - 4) * 32 + h) * 4));
    if (i == 1) MARK(4);
    const int so = qkx_off(s, h, pm) ^ 8192;
    if (i == 0) { ST128(o0, ov, so); ST128(o1, ov + 256, so); }
    if (s < 62) x_issue(rx, w, xv, s + 2, (i & 1) ? LDS_X0 : LDS_X1);
    if (i == 0) continue;
    proj_tile(w0, w1, w2, w3, w4, ((i & 1) ? LDS_X1 : LDS_X0) + lane * 16, o0, o1);
    ST128(o0, ov, so);
    ST128(o1, ov + 256, so);
  }
  asm volatile("s_waitcnt vmcnt(0)\n\ts_barrier" ::: "memory");
  if (tid < 2) ST32(0u, tid * 256, OFF_FQ + ((60 + par) * 32 + h) * 4);
  return pm;
}

DEV PM role_vzb(const Args& a, int h, int par) {
  const int tid = threadIdx.x, lane = tid & 63, w = __builtin_amdgcn_readfirstlane(tid >> 6);
  const rsrc_t rb = __builtin_amdgcn_make_buffer_rsrc(a.ws, 0, 0x0fc00000, 0x00020000);
  const v4i rx = mkrs(a.x, 2048 * 1280), rs = mkrs(a.ws, 0x0fc00000);
  const int g = lane >> 4, n = lane & 15;
  const int xv = x_lane(w, lane), vv = LDS_V + (4 * (w & 3) + g) * 512 + n * 16;
  x_issue(rx, w, xv, 0, LDS_X0);
  v8u W[2][10];
  {
    const v4i rv = mkrs(a.wv, 640 * 8192), ro = mkrs(a.wo, 4096 * 1280);
    const StA st = stA_init(w, lane, h);
    const StB sb = stB_init(w, lane, h);
    const int b0 = stA_base(lane, 4 * (w & 3)), b1 = stA_base(lane, 4 * (w & 3) + 2), r0 = 64 * (w >> 2);
    Acc t = {};
    stA_issue(st, rv, r0, 0); stA_issue(st, rv, 128 + r0, STAGE_BUF);
#pragma unroll
    for (int r = 0; r < 5; r++) {
      if (r < 4) STAGE_WAIT(4); else STAGE_WAIT(5);
      if (w < 4) {
        W[0][2 * r] = stA_read(b0, (r & 1) * STAGE_BUF); W[0][2 * r + 1] = stA_read(b0, (r & 1) * STAGE_BUF + 16384);
        W[1][2 * r] = stA_read(b1, (r & 1) * STAGE_BUF); W[1][2 * r + 1] = stA_read(b1, (r & 1) * STAGE_BUF + 16384);
      }
      BAR();
      if (r + 2 < 5) stA_issue(st, rv, 128 * (r + 2) + r0, (r & 1) * STAGE_BUF); else stB_issue(sb, ro, w, r - 3, r & 1);
      if (w < 4) { first_step(t, W[0][2 * r], W[1][2 * r], 2 * r, lane); first_step(t, W[0][2 * r + 1], W[1][2 * r + 1], 2 * r + 1, lane); }
    }
    if (w < 4) { *(LAS v4u*)(size_t)(vv + 8192) = first_out(t.c[0], t.c[1]); *(LAS v4u*)(size_t)(vv + 8192 + 256) = first_out(t.c[2], t.c[3]); }
    const int q = (lane >> 2) & 3, row = 4 * g + (q >> 1) + 16 * (q & 1);
#pragma unroll
    for (int r = 0; r < 4; r++) {
      STAGE_WAIT(r == 0 ? 5 : r == 1 ? 10 : r == 2 ? 5 : 0);
      if (w >= 4) {
#pragma unroll
        for (int t = 0; t < 10; t++) {
          const int ad = stB_addr(row, 20 * (w & 3) + 2 * t, lane) + ((r + 1) & 1) * STAGE_BUF;
          const v4u x = cat4(ldstr(ad), ldstr(ad + 2560));
          W[r >> 1][t] = (r & 1) ? cat8(lo4(W[r >> 1][t]), x) : cat8(x, x);
        }
      }
      BAR();
      if (r + 2 < 4) stB_issue(sb, ro, w, r + 2, (r + 1) & 1);
      if (r == 0) x_issue(rx, w, xv, 1, LDS_X0);
    }
  }
  MARK(1);
  W_CAT;
  const v4u pv = pol_load(a);
  PM pm = 0;
#pragma unroll 1
  for (int i = 0; i <= 32; i++) {
    const int s = 2 * i + par;
    if (i < 8 || w < 4) asm volatile("s_waitcnt vmcnt(0) lgkmcnt(0)\n\ts_barrier" ::: "memory"); else asm volatile("s_waitcnt vmcnt(10) lgkmcnt(0)\n\ts_barrier" ::: "memory");
    if (i == 1) pm = pol_get(a, pv);
    if (i > 0 && i != 8 && tid == 0) ST32(0u, 0, UNI((i < 3 ? (par ? OFF_FY : OFF_FZ) + (i - 1) * 128 : OFF_FZ + (i < 8 ? s - 4 : s - 6) * 128) + h * 4));
    if (i == 1) MARK(4);
    if (i < 31) x_issue(rx, w, xv, s + 2, (i & 1) ? LDS_X0 : LDS_X1);
    if (w < 4) {
      if (i < 32) {
        v4u o0, o1;
        proj_tile(w0, w1, w2, w3, w4, ((i & 1) ? LDS_X1 : LDS_X0) + lane * 16, o0, o1);
        *(LAS v4u*)(size_t)(vv + (i & 1) * 8192) = o0;
        *(LAS v4u*)(size_t)(vv + (i & 1) * 8192 + 256) = o1;
      }
    } else {
      const int vb = LDS_V + ((i - 1) & 1) * 8192 + g * 512 + n * 16, so = UNI(zx_off(i < 2 ? i : s - 2, (i < 2 ? 5 * par : 0) + 10 * (w & 3), h));
      if (i >= 2) z_tiles(w0, w1, w2, w3, w4, vb, lane * 16, rs, so);
      else if (par) z_half<5>(w0, w1, w2, w3, w4, vb, lane * 16, rs, so);
      else z_half<0>(w0, w1, w2, w3, w4, vb, lane * 16, rs, so);
    }
  }
  asm volatile("s_waitcnt vmcnt(0)\n\ts_barrier" ::: "memory");
  if (tid < 2) ST32(0u, tid * 256, OFF_FZ + ((60 + par) * 32 + h) * 4);
  return pm;
}

DEV void all_flags(rsrc_t rb, int s, int& budget) {
  for (;;) {
    FENCE();
    const v2u f = LD64((threadIdx.x & 63) * 8, OFF_FP + s * 512);
    if (!any(max(f.x, f.y) == 0xFFFFFFFFu) || --budget < 0) break;
    __builtin_amdgcn_s_sleep(OP);
  }
}
DEV void o_tasks(const Args& a, int e, PM pmask, u32 tz0) {
  const int tid = threadIdx.x, lane = tid & 63, w = __builtin_amdgcn_readfirstlane(tid >> 6);
  const rsrc_t rb = __builtin_amdgcn_make_buffer_rsrc(a.ws, 0, 0x0fc00000, 0x00020000);
  const rsrc_t ro = __builtin_amdgcn_make_buffer_rsrc(a.out, 0, 2048 * 1280, 0x00020000);
  int budget = BUDGET;
  for (int id = e; id < 620; id = id < 384 ? id + 128 : id < 512 && e >= 20 ? id + 108 : 620) {
    const int s = id / 10, j = id - s * 10, po = pt_off(s, pmask);
    wait_until(tz0 + OT + (id * OD >> 4));
    poll32(rb, (lane & 3) * 4 + (s < 2 ? (lane >> 5) * (OFF_FY - OFF_FZ) : 0), OFF_FZ + s * 128 + w * 16, budget);
    v4u zx[4][4], zm[4], pt[4][2], pm[2];
#pragma unroll
    for (int c = 0; c < 4; c++) {
#pragma unroll
      for (int hh = 0; hh < 4; hh++) zx[c][hh] = LD128(lane * 16 + hh * 1024, zx_off(s, 4 * j + c, 4 * w));
      if (id < OL) VMW(0);
    }
    if (s < OQ) poll32<OP>(rb, (lane & 15) * 4, OFF_FP + s * 512 + w * 64, budget);
    else {
      if (w == 0) {
        const int b0 = budget, d = j * (s == 61 ? OJ / 2 : OJ);
        wait_until(tz0 + O1 + s * 180 + d);
        all_flags(rb, s, budget);
        if (budget != b0) wait_until(rt32() + d);
      }
      asm volatile("s_barrier" ::: "memory");
    }
#pragma unroll
    for (int hh = 0; hh < 4; hh++) { pt[hh][0] = LD128(lane * 16, po + w * 32768 + hh * 2048); pt[hh][1] = LD128(lane * 16 + 1024, po + w * 32768 + hh * 2048); if (id < OL && (hh & 1)) VMW(0); }
    pm[0] = LD128(lane * 16, po + (8 + (w >> 2)) * 32768 + (w & 3) * 2048); pm[1] = LD128(lane * 16 + 1024, po + (8 + (w >> 2)) * 32768 + (w & 3) * 2048);
    for (;;) {
      FENCE();
      u32 m = 0;
#pragma unroll
      for (int c = 0; c < 4; c++) { zm[c] = LD128(lane * 16, zm_off(s, pmask) + (4 * j + c) * 16384 + w * 1024); m = umax3(m, zm[c].x, zm[c].y); m = umax3(m, zm[c].z, zm[c].w); }
      if (!any(m == 0xFFFFFFFFu) || --budget < 0) break;
    }
#pragma unroll
    for (int c = 0; c < 4; c++)
#pragma unroll
      for (int tt = 0; tt < 2; tt++) {
        f4 acc = {0, 0, 0, 0};
#pragma unroll
        for (int hh = 0; hh < 4; hh++) acc = MF16(zx[c][hh], pt[hh][tt], acc);
        acc = MF16(zm[c], pm[tt], acc);
        *(LAS f4*)(size_t)(w * 8192 + tt * 4096 + (lane & 15) * 256 + (((c * 4 + (lane >> 4)) ^ (lane & 15)) << 4)) = acc;
      }
    BAR();
    const int row = 4 * w + (lane >> 4), ra = row * 256 + (((lane ^ row) & 15) << 4);
    f4 r = *(const LAS f4*)(size_t)ra;
#pragma unroll
    for (int k = 1; k < 8; k++) r += *(const LAS f4*)(size_t)(k * 8192 + ra);
    __builtin_amdgcn_raw_buffer_store_b64((v2u){pkbf(r[0], r[1]), pkbf(r[2], r[3])}, ro, row * 1280 + (lane & 15) * 8, s * 40960 + j * 128, OAUX);
    BAR();
  }
  if (e < 80) {
    const int s = e < 40 ? 63 : 62, c0 = (e < 40 ? e : e - 40) & ~1, tt = e & 1, po = pt_off(s, pmask);
    v4u zx[2][4], zm[2], pt[4], pm;
    poll32(rb, (lane & 3) * 4, OFF_FZ + s * 128 + w * 16, budget);
#pragma unroll
    for (int c = 0; c < 2; c++)
#pragma unroll
      for (int hh = 0; hh < 4; hh++) zx[c][hh] = LD128(lane * 16 + hh * 1024, zx_off(s, c0 + c, 4 * w));
    v8u fl = {0, 0, 0, 0, 0, 0, 0, 0};
    if (w == 0) {
      all_flags(rb, 50, budget);
      wait_until(tz0 + O1 + 61 * 180);
      poll32<OP>(rb, (lane & 31) * 4, OFF_FP + 61 * 512 + (e & 3) * 128, budget);
    }
    if (w == 0 && s == 63) {
      if (OW) { const u32 t = rt32() + OW; while ((int)(t - rt32()) > 0) __builtin_amdgcn_s_sleep(1); }
#define O_LD(i) "buffer_load_dword v[" #i "], %[vo], %[rs], %[so] offen sc1\n\t"
#define O_CK(i) "s_waitcnt vmcnt(3)\n\tv_cmp_eq_u32 vcc, -1, v[" #i "]\n\ts_cbranch_vccz 2f\n\t" O_LD(i)
#define O_SL "s_sleep " STR(OS) "\n\t"
      asm volatile(O_LD(200) O_SL O_LD(202) O_SL O_LD(204) O_SL O_LD(206)
        "1:\n\t" O_CK(200) O_CK(202) O_CK(204) O_CK(206) "s_sub_u32 %[bd], %[bd], 1\n\ts_cmp_lt_i32 %[bd], 0\n\ts_cbranch_scc0 1b\n\t2:"
        : "+{v[200:207]}"(fl), [bd] "+s"(budget) : [vo] "v"((lane & 31) * 4), [rs] "s"(mkrs(a.ws, 0x0fc00000)), [so] "s"(UNI(OFF_FP + 62 * 512 + (e & 3) * 128)) : "vcc", "scc", "memory");
    }
    asm volatile("s_barrier" ::: "memory");
    if (OX) __builtin_amdgcn_s_sleep(OX);
    MARK(6);
    for (;;) {
      FENCE();
      u32 m = 0;
#pragma unroll
      for (int hh = 0; hh < 4; hh++) pt[hh] = LD128(lane * 16, po + w * 32768 + hh * 2048 + tt * 1024);
      pm = LD128(lane * 16, po + (8 + (w >> 2)) * 32768 + (w & 3) * 2048 + tt * 1024);
#pragma unroll
      for (int c = 0; c < 2; c++) zm[c] = LD128(lane * 16, zm_off(s, pmask) + (c0 + c) * 16384 + w * 1024);
#pragma unroll
      for (int hh = 0; hh < 4; hh++) { m = umax3(m, pt[hh].x, pt[hh].y); m = umax3(m, pt[hh].z, pt[hh].w); }
      m = umax3(m, pm.x, pm.y); m = umax3(m, pm.z, pm.w);
#pragma unroll
      for (int c = 0; c < 2; c++) { m = umax3(m, zm[c].x, zm[c].y); m = umax3(m, zm[c].z, zm[c].w); }
      if (!any(m == 0xFFFFFFFFu) || --budget < 0) break;
    }
    asm volatile("" :: "{v[200:207]}"(fl));
    MARK(7);
#pragma unroll
    for (int c = 0; c < 2; c++) {
      f4 acc = {0, 0, 0, 0}, ac2 = {0, 0, 0, 0};
      acc = MF16(zx[c][0], pt[0], acc); ac2 = MF16(zx[c][1], pt[1], ac2);
      acc = MF16(zx[c][2], pt[2], acc); ac2 = MF16(zx[c][3], pt[3], ac2);
      acc = MF16(zm[c], pm, acc);
      *(LAS f4*)(size_t)(w * 8192 + c * 1024 + lane * 16) = acc + ac2;
    }
    BAR();
    if (w < 2) {
      f4 r = *(const LAS f4*)(size_t)(w * 1024 + lane * 16);
#pragma unroll
      for (int k = 1; k < 8; k++) r += *(const LAS f4*)(size_t)(k * 8192 + w * 1024 + lane * 16);
      __builtin_amdgcn_raw_buffer_store_b64((v2u){pkbf(r[0], r[1]), pkbf(r[2], r[3])}, ro, (lane & 15) * 1280 + (lane >> 4) * 8, s * 40960 + tt * 20480 + (c0 + w) * 32, OAUX2);
    }
    MARK(3);
    __builtin_amdgcn_endpgm();
  }
}

__global__ void __launch_bounds__(512, 2) k_all(Args a) {
  const int b = blockIdx.x, x = b & 7, r = b >> 3;
  MARK(0);
  if (x < 4) {
    if (r < 8) role_qk(a, x * 8 + r);
    else if (r < 16) role_vo(a, x * 8 + r - 8);
    else if (r < 26) role_z(a, x * 10 + r - 16);
    else clear_words(a, x * 6 + r - 26);
  } else {
    const int h = (x - 4) * 8 + ((r & 15) >> 1);
    const u32 tz0 = rt32();
    const PM pm = r < 16 ? role_qkb(a, h, r & 1) : role_vzb(a, h, r & 1);
    MARK(2);
    o_tasks(a, r * 4 + x - 4, pm, tz0);
  }
  MARK(3);
}

extern "C" void kernel_launch(void* const* d_in, const int* in_sizes, int n_in, void* d_out, int out_size, void* d_ws, size_t ws_size, hipStream_t stream) {
  unsigned char* ws = (unsigned char*)((((uintptr_t)d_ws + OFF_ZM + 2097151) & ~(uintptr_t)2097151) - OFF_ZM);
  Args a{(const u16*)d_in[0], (const u16*)d_in[1], (const u16*)d_in[2], (const u16*)d_in[3], (const u16*)d_in[4], (const u16*)d_in[5], (u16*)d_out, ws};
  (void)hipFuncSetAttribute((const void*)k_all, hipFuncAttributeMaxDynamicSharedMemorySize, LDS_BYTES);
  k_all<<<256, 512, LDS_BYTES, stream>>>(a);
}
```
